```python
import torch

def _softmax(x, axis=-1):
    return torch.softmax(x, dim=axis)
def _gelu(x):
    return torch.nn.functional.gelu(x, approximate="tanh")
def _silu(x):
    return torch.nn.functional.silu(x)
def _sigmoid(x):
    return torch.sigmoid(x)
def _layernorm(x, axis=-1, eps=1e-5):
    m = x.mean(dim=axis, keepdim=True)
    v = x.var(dim=axis, keepdim=True, unbiased=False)
    return (x - m) / torch.sqrt(v + eps)
def _scatter_add(tgt, idx, src, axis=0):
    return tgt.clone().index_add_(axis, idx, src)
def _segment_sum(src, seg, n):
    out = torch.zeros((n,) + src.shape[1:], dtype=src.dtype)
    return out.index_add_(0, seg, src)
def _pad(x, pw):
    flat = tuple(v for pair in reversed(pw) for v in pair)
    return torch.nn.functional.pad(x, flat)

kernel_name = 'sequential_preLN__grouped_kv_mqa__geglu_ffn__prompt'

def setup_inputs():

    _w = (lambda a: (lambda t: t.to(torch.bfloat16) if t.is_floating_point() else t)(torch.as_tensor(a)))
    def _A_setup():
        B, S, D, H = 1, 2048, 640, 32
        Dh = D // H
        return {
            "x": _w((lambda *s: torch.randn(*s, dtype=torch.bfloat16) * 0.05)(B, S, D)),
            "wq": _w((lambda *s: torch.randn(*s, dtype=torch.bfloat16) * 0.05)(D, H * Dh)),
            "wk": _w((lambda *s: torch.randn(*s, dtype=torch.bfloat16) * 0.05)(D, Dh)),
            "wv": _w((lambda *s: torch.randn(*s, dtype=torch.bfloat16) * 0.05)(D, Dh)),
            "wo": _w((lambda *s: torch.randn(*s, dtype=torch.bfloat16) * 0.05)(H * Dh, D)),
        }
    _A_in = _A_setup()

    def _B_setup():
        B, S, D, F = 1, 2048, 640, 1536
        return {
            "x": _w((lambda *s: torch.randn(*s, dtype=torch.bfloat16) * 0.05)(B, S, D)),
            "w_gate": _w((lambda *s: torch.randn(*s, dtype=torch.bfloat16) * 0.05)(D, F)),
            "w_up": _w((lambda *s: torch.randn(*s, dtype=torch.bfloat16) * 0.05)(D, F)),
            "w_down": _w((lambda *s: torch.randn(*s, dtype=torch.bfloat16) * 0.05)(F, D)),
        }
    _B_in = _B_setup()

    return {
        "A_x": _A_in["x"],
        "A_wq": _A_in["wq"],
        "A_wk": _A_in["wk"],
        "A_wv": _A_in["wv"],
        "A_wo": _A_in["wo"],
        "B_x": _B_in["x"],
        "B_w_gate": _B_in["w_gate"],
        "B_w_up": _B_in["w_up"],
        "B_w_down": _B_in["w_down"],
    }

def reference(A_x, A_wq, A_wk, A_wv, A_wo, B_x, B_w_gate, B_w_up, B_w_down):
    def _A_ref(x, wq, wk, wv, wo):
        B, S, D = x.shape
        H = 32
        Dh = D // H
        q = (lambda x, *d: x.permute(*d))((x @ wq).reshape(B, S, H, Dh), 0, 2, 1, 3)
        k = x @ wk
        v = x @ wv
        scores = torch.einsum("bhqd,bkd->bhqk", q, k) * (Dh ** -0.5)
        p = _softmax(scores, axis=-1)
        o = torch.einsum("bhqk,bkd->bhqd", p, v)
        return (lambda x, *d: x.permute(*d))(o, 0, 2, 1, 3).reshape(B, S, H * Dh) @ wo

    def _B_ref(x, w_gate, w_up, w_down):
        return (_gelu(x @ w_gate) * (x @ w_up)) @ w_down

    x = A_x
    h = x + _A_ref(_layernorm(x, axis=-1), A_wq, A_wk, A_wv, A_wo)
    h2 = h + _B_ref(_layernorm(h, axis=-1), B_w_gate, B_w_up, B_w_down)
    return h2
```

```cpp
#include <hip/hip_runtime.h>
#include <hip/hip_bf16.h>
#include <stdint.h>

typedef __attribute__((ext_vector_type(8))) short bf16x8;
typedef __attribute__((ext_vector_type(4))) short s16x4;
typedef __attribute__((ext_vector_type(4))) float f32x4;
typedef __attribute__((ext_vector_type(2))) float f32x2;
typedef __attribute__((ext_vector_type(16))) float f32x16;
typedef __attribute__((ext_vector_type(2))) __bf16 bf16x2_t;
typedef __attribute__((ext_vector_type(4))) unsigned u32x4;
typedef __attribute__((ext_vector_type(2))) unsigned u32x2;
typedef unsigned short u16;
#define LAS __attribute__((address_space(3)))
#define DI __device__ __forceinline__

constexpr int S_ = 2048, D_ = 640, F_ = 1536;
constexpr float QSCALE = 0.22360679774997896f * 1.4426950408889634f;
constexpr float LN_EPS = 1e-5f;
constexpr float FAST_MAX = 40.0f;

constexpr size_t WS_Q = 0;
constexpr size_t WS_KF = WS_Q + 2048 * 640 * 2;
constexpr size_t WS_VF = WS_KF + 64 * 2048;
constexpr size_t WS_KMX = WS_VF + 64 * 2048;
constexpr size_t WS_CS = WS_KMX + 256;
constexpr size_t WS_O = WS_CS + 3072 * 4;
constexpr size_t WS_H = WS_O + 2048 * 640 * 2;
constexpr size_t WS_ST = WS_H + 2048 * 640 * 2;
constexpr size_t WS_G = WS_ST + 2048 * 16 * 4;

DI unsigned pk_bf16(float a, float b) {
  f32x2 v = {a, b};
  return __builtin_bit_cast(unsigned, __builtin_convertvector(v, bf16x2_t));
}
DI float bf_lo(unsigned u) { return __uint_as_float(u << 16); }
DI float bf_hi(unsigned u) { return __uint_as_float(u & 0xffff0000u); }
DI void glds16(const void* g, char LAS* l) {
  __builtin_amdgcn_global_load_lds((const unsigned*)g, (unsigned LAS*)l, 16, 0, 0);
}
DI s16x4 tr_read(const char LAS* p) { return __builtin_amdgcn_ds_read_tr16_b64_v4i16((s16x4 LAS*)p); }
#define MFMA16(a, b, c) __builtin_amdgcn_mfma_f32_16x16x32_bf16((a), (b), (c), 0, 0, 0)
#define MFMA32(a, b, c) __builtin_amdgcn_mfma_f32_32x32x16_bf16((a), (b), (c), 0, 0, 0)
#define WAIT_V(n) asm volatile("s_waitcnt vmcnt(%0)" ::"i"(n) : "memory")
#define BAR() __builtin_amdgcn_s_barrier()

template <int BN> struct BMap;
template <> struct BMap<80> {
  static DI int prow(int k) { return k ^ (((k >> 3) & 1) << 2); }
  static DI int fseg(int k) { return 0; }
};
template <> struct BMap<96> {
  static DI int prow(int k) { return k; }
  static DI int fseg(int k) { return (k >> 3) & 1; }
};
template <> struct BMap<192> {
  static DI int prow(int k) { return k; }
  static DI int fseg(int k) { return ((k >> 1) & 1) | (((k >> 3) & 1) << 1); }
};

template <int BN> DI int boff_lo(int cb, int lane) {
  const int g = lane >> 4, q = (lane >> 2) & 3, p = lane & 3, k = 8 * g + q;
  return BMap<BN>::prow(k) * (2 * BN) + ((cb ^ BMap<BN>::fseg(k)) * 32) + p * 8;
}
template <int BN> DI int boff_hi(int cb, int lane) {
  const int g = lane >> 4, q = (lane >> 2) & 3, p = lane & 3, k = 8 * g + q + 4;
  return BMap<BN>::prow(k) * (2 * BN) + ((cb ^ BMap<BN>::fseg(k)) * 32) + p * 8;
}
DI bf16x8 read_b2(const char LAS* plo, const char LAS* phi) {
  s16x4 lo = tr_read(plo), hi = tr_read(phi);
  return __builtin_shufflevector(lo, hi, 0, 1, 2, 3, 4, 5, 6, 7);
}
DI bf16x8 read_afrag(const char LAS* img, int row, int c) {
  return *(const bf16x8 LAS*)(img + row * 128 + ((c ^ ((row >> 1) & 7)) * 16));
}
DI bf16x8 ones8() {
  bf16x8 o;
#pragma unroll
  for (int j = 0; j < 8; ++j) o[j] = 0x3F80;
  return o;
}
template <int N> DI void wait_stage(int pend) {
  if (pend >= 3) WAIT_V(3 * N);
  else if (pend == 2) WAIT_V(2 * N);
  else if (pend == 1) WAIT_V(N);
  else WAIT_V(0);
}

template <int BN, int NST, int NIT, bool STATS>
struct SmallGemm {
  static constexpr int CPR = BN / 8;
  static constexpr int BPIECES = 128 * BN * 2 / 1024;
  static constexpr int NI = (16 + BPIECES) / 4;
  static constexpr int STAGE = (16 + BPIECES) * 1024;
  static constexpr int NCB = BN / 16;

  static DI void run(char LAS* lds, const u16* A, int lda, int row0, const u16* B, int ldb, int n0, int nmax,
                     int rot, f32x4 (&acc)[NCB], f32x4& rs, f32x4& sq, f32x4& cs0, f32x4& cs1) {
    const int tid = threadIdx.x, lane = tid & 63;
    const int w = __builtin_amdgcn_readfirstlane(tid >> 6);
    const char* gp[NI];
#pragma unroll
    for (int i = 0; i < NI; ++i) {
      const int pi = i * 4 + w;
      if (i < 4) {
        const int sub = pi >> 3, row = 8 * (pi & 7) + (lane >> 3);
        const int c = (lane & 7) ^ ((row >> 1) & 7);
        gp[i] = (const char*)(A + (size_t)(row0 + row) * lda + sub * 64 + c * 8);
      } else {
        const int ci = (pi - 16) * 64 + lane;
        const int pr = ci / CPR, pc = ci % CPR;
        const int k = BMap<BN>::prow(pr);
        const int c = pc ^ (BMap<BN>::fseg(k) << 1);
        int n = n0 + c * 8;
        n = n > nmax - 8 ? nmax - 8 : n;
        gp[i] = (const char*)(B + (size_t)k * ldb + n);
      }
    }
    const int bstep = 128 * ldb * 2;
    auto issue = [&](int st) {
      char LAS* dst = lds + (st % NST) * STAGE + w * 1024;
#pragma unroll
      for (int i = 0; i < NI; ++i)
        glds16(gp[i] + (size_t)st * (i < 4 ? 256 : bstep), dst + i * 4096);
    };
#pragma unroll
    for (int s = 0; s < NST - 1; ++s) issue(s);
    const int arow = 16 * w + (lane & 15), g = lane >> 4;
    int blo[NCB], bhi[NCB];
#pragma unroll
    for (int p = 0; p < NCB; ++p) {
      int cb = p + rot;
      cb = cb >= NCB ? cb - NCB : cb;
      blo[p] = boff_lo<BN>(cb, lane);
      bhi[p] = boff_hi<BN>(cb, lane);
    }
    const bf16x8 ones = ones8();
#pragma unroll
    for (int t = 0; t < NIT; ++t) {
      const int issued = (t + NST - 1 < NIT) ? (t + NST - 1) : NIT;
      wait_stage<NI>(issued - t - 1);
      BAR();
      if (t + NST - 1 < NIT) issue(t + NST - 1);
      const char LAS* sa = lds + (t % NST) * STAGE;
      const char LAS* sb = sa + 16384;
#pragma unroll
      for (int kk = 0; kk < 4; ++kk) {
        const bf16x8 af = read_afrag(sa + (kk >> 1) * 8192, arow, (kk & 1) * 4 + g);
        if (STATS) {
          rs = MFMA16(ones, af, rs);
          sq = MFMA16(af, af, sq);
        }
#pragma unroll
        for (int p = 0; p < NCB; ++p) {
          const bf16x8 bfr = read_b2(sb + kk * 32 * 2 * BN + blo[p], sb + kk * 32 * 2 * BN + bhi[p]);
          acc[p] = MFMA16(bfr, af, acc[p]);
          if (STATS && p == 0) cs0 = MFMA16(bfr, ones, cs0);
          if (STATS && p == 4) cs1 = MFMA16(bfr, ones, cs1);
        }
      }
    }
  }
};

DI void finish_stats(const f32x4& rs, const f32x4& sq, int lane, float& mean, float& rstd) {
  const int m = lane & 15;
  const int r = m & 3;
  float d = r == 0 ? sq[0] : (r == 1 ? sq[1] : (r == 2 ? sq[2] : sq[3]));
  d = __shfl(d, (m >> 2) * 16 + m);
  mean = rs[0] * (1.0f / 640.0f);
  float var = d * (1.0f / 640.0f) - mean * mean;
  var = var < 0.f ? 0.f : var;
  rstd = 1.0f / sqrtf(var + LN_EPS);
}

constexpr int K1_LDS = 3 * 40960 + 1024;
__global__ __launch_bounds__(256, 1) void k1_qkv(const u16* __restrict__ x, const u16* __restrict__ wq,
                                                  const u16* __restrict__ wk, const u16* __restrict__ wv,
                                                  const u16* __restrict__ wg, const u16* __restrict__ wu,
                                                  char* __restrict__ ws) {
  extern __shared__ __attribute__((aligned(16))) char smem_[];
  char LAS* lds = (char LAS*)smem_;
  const int tid = threadIdx.x, lane = tid & 63;
  const int w = __builtin_amdgcn_readfirstlane(tid >> 6);
  const int bid = blockIdx.x, xcd = bid & 7, idx = bid >> 3;
  const int rt = xcd * 4 + (idx & 3), ct = idx >> 2;
  const int row0 = rt * 64;
  const int g = lane >> 4, m = lane & 15;
  f32x4 z4 = {0.f, 0.f, 0.f, 0.f};
  f32x4 rs = z4, sq = z4;
  float LAS* cs_l = (float LAS*)(lds + 3 * 40960);
  if (ct < 7) {
    using G = SmallGemm<96, 3, 5, true>;
    f32x4 acc[6], cs0 = z4, cs1 = z4;
#pragma unroll
    for (int p = 0; p < 6; ++p) acc[p] = z4;
    G::run(lds, x, D_, row0, wq, D_, ct * 96, D_, w, acc, rs, sq, cs0, cs1);
    float mean, rstd;
    finish_stats(rs, sq, lane, mean, rstd);
    if (m == 0) {
      *(f32x4 LAS*)(cs_l + w * 16 + 4 * g) = cs0;
      if (w < 2) *(f32x4 LAS*)(cs_l + (4 + w) * 16 + 4 * g) = cs1;
    }
    __syncthreads();
    u16* q = (u16*)(ws + WS_Q);
    const float sc = rstd * QSCALE;
#pragma unroll
    for (int p = 0; p < 6; ++p) {
      int cb = p + w;
      cb = cb >= 6 ? cb - 6 : cb;
      const int n = ct * 96 + cb * 16 + 4 * g;
      const f32x4 c = *(const f32x4 LAS*)(cs_l + cb * 16 + 4 * g);
      if (n < D_) {
        u32x2 o;
        o[0] = pk_bf16((acc[p][0] - mean * c[0]) * sc, (acc[p][1] - mean * c[1]) * sc);
        o[1] = pk_bf16((acc[p][2] - mean * c[2]) * sc, (acc[p][3] - mean * c[3]) * sc);
        *(u32x2*)(q + (size_t)(row0 + 16 * w + m) * D_ + n) = o;
      }
    }
  } else {
    constexpr int NI = 7, STAGE = 28 * 1024, NST = 4, NIT = 5;
    const char* gp[NI];
#pragma unroll
    for (int i = 0; i < NI; ++i) {
      const int pi = i * 4 + w;
      if (i < 4) {
        const int sub = pi >> 3, row = 8 * (pi & 7) + (lane >> 3);
        const int c = (lane & 7) ^ ((row >> 1) & 7);
        gp[i] = (const char*)(x + (size_t)(row0 + row) * D_ + sub * 64 + c * 8);
      } else {
        int pj = pi - 16;
        pj = pj >= 10 ? pj - 10 : pj;
        const u16* src = pj < 5 ? wk : wv;
        gp[i] = (const char*)src + (pj < 5 ? pj : pj - 5) * 1024 + lane * 16;
      }
    }
    auto issue = [&](int st) {
      char LAS* dst = lds + (st % NST) * STAGE + w * 1024;
#pragma unroll
      for (int i = 0; i < NI; ++i) glds16(gp[i] + (size_t)st * (i < 4 ? 256 : 5120), dst + i * 4096);
    };
#pragma unroll
    for (int s = 0; s < NST - 1; ++s) issue(s);
    const int arow = 16 * w + m;
    const int q4 = (lane >> 2) & 3, p4 = lane & 3;
    const int bo = (8 * g + q4) * 40 + p4 * 8;
    f32x4 ak0 = z4, ak1 = z4, av0 = z4, av1 = z4, ck0 = z4, ck1 = z4, cv0 = z4, cv1 = z4;
    const bf16x8 ones = ones8();
#pragma unroll
    for (int t = 0; t < NIT; ++t) {
      const int issued = (t + NST - 1 < NIT) ? (t + NST - 1) : NIT;
      wait_stage<NI>(issued - t - 1);
      BAR();
      if (t + NST - 1 < NIT) issue(t + NST - 1);
      const char LAS* sa = lds + (t % NST) * STAGE;
      const char LAS* sk = sa + 16384;
      const char LAS* sv = sk + 5120;
#pragma unroll
      for (int kk = 0; kk < 4; ++kk) {
        const bf16x8 af = read_afrag(sa + (kk >> 1) * 8192, arow, (kk & 1) * 4 + g);
        rs = MFMA16(ones, af, rs);
        sq = MFMA16(af, af, sq);
        const int o = kk * 32 * 40 + bo;
        const bf16x8 k0 = read_b2(sk + o, sk + o + 160), k1 = read_b2(sk + o + 32, sk + o + 192);
        const bf16x8 v0 = read_b2(sv + o, sv + o + 160), v1 = read_b2(sv + o + 32, sv + o + 192);
        ak0 = MFMA16(k0, af, ak0);
        ak1 = MFMA16(k1, af, ak1);
        ck0 = MFMA16(k0, ones, ck0);
        ck1 = MFMA16(k1, ones, ck1);
        av0 = MFMA16(af, v0, av0);
        av1 = MFMA16(af, v1, av1);
        cv0 = MFMA16(ones, v0, cv0);
        cv1 = MFMA16(ones, v1, cv1);
      }
    }
    float mean, rstd;
    finish_stats(rs, sq, lane, mean, rstd);
    u16* kf = (u16*)(ws + WS_KF);
    u16* vf = (u16*)(ws + WS_VF);
    const int key = row0 + 16 * w + m;
    const int blk = key >> 5, kk5 = key & 31;
    const int r = (kk5 & 19) | ((kk5 & 4) << 1) | ((kk5 & 8) >> 1);
    float kv[4], n2;
    u32x2 o;
#pragma unroll
    for (int j = 0; j < 4; ++j) kv[j] = (ak0[j] - mean * ck0[j]) * rstd;
    o[0] = pk_bf16(kv[0], kv[1]);
    o[1] = pk_bf16(kv[2], kv[3]);
    n2 = bf_lo(o[0]) * bf_lo(o[0]) + bf_hi(o[0]) * bf_hi(o[0]) + bf_lo(o[1]) * bf_lo(o[1]) + bf_hi(o[1]) * bf_hi(o[1]);
    *(u32x2*)(kf + (size_t)blk * 1024 + ((g >> 1) * 32 + r) * 8 + (g & 1) * 4) = o;
    {
      u32x2 o1;
#pragma unroll
      for (int j = 0; j < 4; ++j) kv[j] = (ak1[j] - mean * ck1[j]) * rstd;
      o1[0] = pk_bf16(kv[0], kv[1]);
      o1[1] = pk_bf16(kv[2], kv[3]);
      if (g == 0)
        n2 += bf_lo(o1[0]) * bf_lo(o1[0]) + bf_hi(o1[0]) * bf_hi(o1[0]) + bf_lo(o1[1]) * bf_lo(o1[1]) +
              bf_hi(o1[1]) * bf_hi(o1[1]);
      if (g == 1) { o1[0] = 0x3F803F80u; o1[1] = 0x00003F80u; }
      if (g >= 2) { o1[0] = 0u; o1[1] = 0u; }
      *(u32x2*)(kf + (size_t)blk * 1024 + 512 + ((g >> 1) * 32 + r) * 8 + (g & 1) * 4) = o1;
    }
    n2 += __shfl_xor(n2, 16);
    n2 += __shfl_xor(n2, 32);
#pragma unroll
    for (int s = 1; s < 16; s <<= 1) n2 = fmaxf(n2, __shfl_xor(n2, s));
    float LAS* red = (float LAS*)(lds + 4 * 28 * 1024);
    if (lane == 0) red[w] = n2;
    {
      float mj[4], rj[4];
#pragma unroll
      for (int j = 0; j < 4; ++j) {
        mj[j] = __shfl(mean, 4 * g + j);
        rj[j] = __shfl(rstd, 4 * g + j);
      }
      const int key0 = row0 + 16 * w + 4 * g;
      const int vb = key0 >> 5, k5 = key0 & 31;
      const int s = k5 >> 4, h = (k5 >> 3) & 1, j0 = k5 & 7;
      u16* base = vf + (size_t)vb * 1024 + s * 512 + h * 256 + j0;
      u32x2 ov;
      ov[0] = pk_bf16((av0[0] - mj[0] * cv0[0]) * rj[0], (av0[1] - mj[1] * cv0[1]) * rj[1]);
      ov[1] = pk_bf16((av0[2] - mj[2] * cv0[2]) * rj[2], (av0[3] - mj[3] * cv0[3]) * rj[3]);
      *(u32x2*)(base + m * 8) = ov;
      ov[0] = pk_bf16((av1[0] - mj[0] * cv1[0]) * rj[0], (av1[1] - mj[1] * cv1[1]) * rj[1]);
      ov[1] = pk_bf16((av1[2] - mj[2] * cv1[2]) * rj[2], (av1[3] - mj[3] * cv1[3]) * rj[3]);
      if (m == 4) { ov[0] = 0x3F803F80u; ov[1] = 0x3F803F80u; }
      if (m > 4) { ov[0] = 0u; ov[1] = 0u; }
      *(u32x2*)(base + (16 + m) * 8) = ov;
    }
    __syncthreads();
    if (tid == 0) ((float*)(ws + WS_KMX))[rt] = fmaxf(fmaxf(red[0], red[1]), fmaxf(red[2], red[3]));
    {
      const int c0 = rt * 96;
      const u16* src = c0 < F_ ? wg + c0 : wu + (c0 - F_);
      float a[8];
#pragma unroll
      for (int j = 0; j < 8; ++j) a[j] = 0.f;
      const int ch = tid % 12, rg = tid / 12;
      if (rg < 20) {
#pragma unroll 8
        for (int i = 0; i < 32; ++i) {
          const u32x4 v = *(const u32x4*)(src + (size_t)(rg * 32 + i) * F_ + ch * 8);
#pragma unroll
          for (int e = 0; e < 4; ++e) {
            a[2 * e] += bf_lo(v[e]);
            a[2 * e + 1] += bf_hi(v[e]);
          }
        }
      }
      float LAS* cred = (float LAS*)lds;
      if (rg < 20) {
#pragma unroll
        for (int j = 0; j < 8; ++j) cred[rg * 96 + ch * 8 + j] = a[j];
      }
      __syncthreads();
      if (tid < 96) {
        float s = 0.f;
#pragma unroll
        for (int i = 0; i < 20; ++i) s += cred[i * 96 + tid];
        ((float*)(ws + WS_CS))[c0 + tid] = s;
      }
    }
  }
}

constexpr int K2_LDS = 4 * 256 * 96 + 4096;
DI void split3(float M, unsigned& s01, unsigned& s2) {
  const float n = -M;
  const unsigned hi = __float_as_uint(n) & 0xffff0000u;
  const float r1 = n - __uint_as_float(hi);
  const unsigned mid = __float_as_uint(r1) & 0xffff0000u;
  const float r2 = r1 - __uint_as_float(mid);
  const unsigned lo = __float_as_uint(r2) & 0xffff0000u;
  s01 = (hi >> 16) | mid;
  s2 = lo >> 16;
}
__global__ __launch_bounds__(512, 2) void k2_attn(const char* __restrict__ wsr, char* __restrict__ ws) {
  extern __shared__ __attribute__((aligned(16))) char smem_[];
  char LAS* lds = (char LAS*)smem_;
  const int tid = threadIdx.x, lane = tid & 63;
  const int w = __builtin_amdgcn_readfirstlane(tid >> 6);
  const int ch = w & 1, ks = w >> 1;
  const int r = lane & 31, h = lane >> 5;
  const int qrow0 = blockIdx.x * 8;
  const u16* q = (const u16*)(wsr + WS_Q);
  const bf16x8* kf = (const bf16x8*)(wsr + WS_KF) + (size_t)ks * 16 * 128 + lane;
  const bf16x8* vf = (const bf16x8*)(wsr + WS_VF) + (size_t)ks * 16 * 128 + lane;
  bf16x8 q0[4], q1[4];
  float qn[4];
#pragma unroll
  for (int gq = 0; gq < 4; ++gq) {
    const u16* src = q + (size_t)(qrow0 + ch * 4 + gq) * D_ + r * 20;
    const u32x2 a = *(const u32x2*)(src + 8 * h), b = *(const u32x2*)(src + 8 * h + 4);
    u32x2 c = *(const u32x2*)(src + 16);
    if (h) { c[0] = 0u; c[1] = 0u; }
    u32x4 f0 = {a[0], a[1], b[0], b[1]};
    u32x4 f1 = {c[0], c[1], 0u, 0u};
    float n2 = 0.f;
#pragma unroll
    for (int e = 0; e < 4; ++e) n2 += bf_lo(f0[e]) * bf_lo(f0[e]) + bf_hi(f0[e]) * bf_hi(f0[e]);
#pragma unroll
    for (int e = 0; e < 2; ++e) n2 += bf_lo(f1[e]) * bf_lo(f1[e]) + bf_hi(f1[e]) * bf_hi(f1[e]);
    n2 += __shfl_xor(n2, 32);
    qn[gq] = n2;
    q0[gq] = __builtin_bit_cast(bf16x8, f0);
    q1[gq] = __builtin_bit_cast(bf16x8, f1);
  }
  float kmx = ((const float*)(wsr + WS_KMX))[r];
#pragma unroll
  for (int s = 1; s < 32; s <<= 1) kmx = fmaxf(kmx, __shfl_xor(kmx, s));
  float M[4];
  bool unsafe = false;
#pragma unroll
  for (int gq = 0; gq < 4; ++gq) {
    M[gq] = sqrtf(qn[gq] * kmx) * 1.001f + 1e-3f;
    unsafe |= !(M[gq] <= FAST_MAX);
  }
  f32x16 z16;
#pragma unroll
  for (int i = 0; i < 16; ++i) z16[i] = 0.f;
  if (__syncthreads_or(unsafe)) {
    float mx[4] = {-INFINITY, -INFINITY, -INFINITY, -INFINITY};
    for (int b = 0; b < 16; ++b) {
      const bf16x8 k0 = kf[b * 128], k1 = kf[b * 128 + 64];
#pragma unroll
      for (int gq = 0; gq < 4; ++gq) {
        f32x16 s = MFMA32(k0, q0[gq], z16);
        s = MFMA32(k1, q1[gq], s);
#pragma unroll
        for (int i = 0; i < 16; ++i) mx[gq] = fmaxf(mx[gq], s[i]);
      }
    }
    float LAS* mred = (float LAS*)lds;
#pragma unroll
    for (int gq = 0; gq < 4; ++gq) {
      mx[gq] = fmaxf(mx[gq], __shfl_xor(mx[gq], 32));
      if (h == 0) mred[ks * 256 + ch * 128 + gq * 32 + r] = mx[gq];
    }
    __syncthreads();
#pragma unroll
    for (int gq = 0; gq < 4; ++gq) {
      const int c = ch * 128 + gq * 32 + r;
      M[gq] = fmaxf(fmaxf(mred[c], mred[256 + c]), fmaxf(mred[512 + c], mred[768 + c]));
    }
    __syncthreads();
  }
#pragma unroll
  for (int gq = 0; gq < 4; ++gq) {
    unsigned s01, s2;
    split3(M[gq], s01, s2);
    u32x4 f1 = __builtin_bit_cast(u32x4, q1[gq]);
    if (h == 0) { f1[2] = s01; f1[3] = s2; }
    q1[gq] = __builtin_bit_cast(bf16x8, f1);
  }
  f32x16 o[4];
#pragma unroll
  for (int gq = 0; gq < 4; ++gq) o[gq] = z16;
  bf16x8 k0 = kf[0], k1 = kf[64], v0 = vf[0], v1 = vf[64];
  for (int b = 0; b < 16; ++b) {
    const int bn = b < 15 ? b + 1 : 15;
    const bf16x8 nk0 = kf[bn * 128], nk1 = kf[bn * 128 + 64], nv0 = vf[bn * 128], nv1 = vf[bn * 128 + 64];
#pragma unroll
    for (int gq = 0; gq < 4; ++gq) {
      f32x16 s = MFMA32(k0, q0[gq], z16);
      s = MFMA32(k1, q1[gq], s);
      u32x4 p0, p1;
#pragma unroll
      for (int i = 0; i < 4; ++i) {
        p0[i] = pk_bf16(__builtin_amdgcn_exp2f(s[2 * i]), __builtin_amdgcn_exp2f(s[2 * i + 1]));
        p1[i] = pk_bf16(__builtin_amdgcn_exp2f(s[8 + 2 * i]), __builtin_amdgcn_exp2f(s[8 + 2 * i + 1]));
      }
      o[gq] = MFMA32(v0, __builtin_bit_cast(bf16x8, p0), o[gq]);
      o[gq] = MFMA32(v1, __builtin_bit_cast(bf16x8, p1), o[gq]);
    }
    k0 = nk0; k1 = nk1; v0 = nv0; v1 = nv1;
  }
  float LAS* red = (float LAS*)lds;
#pragma unroll
  for (int gq = 0; gq < 4; ++gq) {
    float LAS* dst = red + ((size_t)ks * 256 + ch * 128 + gq * 32 + r) * 24 + 4 * h;
#pragma unroll
    for (int t = 0; t < 3; ++t) {
      f32x4 v = {o[gq][4 * t], o[gq][4 * t + 1], o[gq][4 * t + 2], o[gq][4 * t + 3]};
      *(f32x4 LAS*)(dst + 8 * t) = v;
    }
  }
  __syncthreads();
  u16* og = (u16*)(ws + WS_O) + (size_t)qrow0 * D_;
  for (int it = tid; it < 1280; it += 512) {
    const int c = it / 5, qd = it % 5;
    f32x4 a = {0.f, 0.f, 0.f, 0.f};
    float l = 0.f;
#pragma unroll
    for (int s = 0; s < 4; ++s) {
      const float LAS* src = red + ((size_t)s * 256 + c) * 24;
      a += *(const f32x4 LAS*)(src + 4 * qd);
      l += src[20];
    }
    const float inv = 1.0f / l;
    u32x2 ov;
    ov[0] = pk_bf16(a[0] * inv, a[1] * inv);
    ov[1] = pk_bf16(a[2] * inv, a[3] * inv);
    *(u32x2*)(og + (size_t)it * 4) = ov;
  }
}

template <int KDIM, bool STATS_OUT>
__global__ __launch_bounds__(256, 1) void k3_proj(const u16* __restrict__ A, const u16* __restrict__ W,
                                                   const u16* __restrict__ res, u16* __restrict__ out,
                                                   float* __restrict__ stats) {
  extern __shared__ __attribute__((aligned(16))) char smem_[];
  char LAS* lds = (char LAS*)smem_;
  const int tid = threadIdx.x, lane = tid & 63;
  const int w = __builtin_amdgcn_readfirstlane(tid >> 6);
  const int bid = blockIdx.x, xcd = bid & 7, idx = bid >> 3;
  const int rt = xcd * 4 + (idx & 3), ct = idx >> 2;
  const int g = lane >> 4, m = lane & 15;
  const int row = rt * 64 + 16 * w + m;
  u32x2 rv[5];
#pragma unroll
  for (int p = 0; p < 5; ++p) rv[p] = *(const u32x2*)(res + (size_t)row * D_ + ct * 80 + p * 16 + 4 * g);
  using G = SmallGemm<80, 4, KDIM / 128, false>;
  f32x4 z4 = {0.f, 0.f, 0.f, 0.f};
  f32x4 acc[5], d0 = z4, d1 = z4, d2 = z4, d3 = z4;
#pragma unroll
  for (int p = 0; p < 5; ++p) acc[p] = z4;
  G::run(lds, A, KDIM, rt * 64, W, D_, ct * 80, D_, 0, acc, d0, d1, d2, d3);
  float s1 = 0.f, s2 = 0.f;
#pragma unroll
  for (int p = 0; p < 5; ++p) {
    u32x2 o;
    o[0] = pk_bf16(bf_lo(rv[p][0]) + acc[p][0], bf_hi(rv[p][0]) + acc[p][1]);
    o[1] = pk_bf16(bf_lo(rv[p][1]) + acc[p][2], bf_hi(rv[p][1]) + acc[p][3]);
    *(u32x2*)(out + (size_t)row * D_ + ct * 80 + p * 16 + 4 * g) = o;
    if (STATS_OUT) {
      const float a = bf_lo(o[0]), b = bf_hi(o[0]), c = bf_lo(o[1]), d = bf_hi(o[1]);
      s1 += (a + b) + (c + d);
      s2 += (a * a + b * b) + (c * c + d * d);
    }
  }
  if (STATS_OUT) {
    s1 += __shfl_xor(s1, 16); s1 += __shfl_xor(s1, 32);
    s2 += __shfl_xor(s2, 16); s2 += __shfl_xor(s2, 32);
    if (g == 0) { f32x2 v = {s1, s2}; *(f32x2*)(stats + ((size_t)row * 8 + ct) * 2) = v; }
  }
}

constexpr int K4_STAGE = 40960, K4_LDS = 3 * K4_STAGE;
DI float gelu_tanh(float x) {
  const float u = 0.7978845608028654f * (x + 0.044715f * x * x * x);
  const float e = __builtin_amdgcn_exp2f(u * 2.8853900817779268f);
  const float t = 1.0f - 2.0f * __builtin_amdgcn_rcpf(e + 1.0f);
  return 0.5f * x * (1.0f + t);
}
__global__ __launch_bounds__(512, 2) void k4_ffn1(const u16* __restrict__ hh, const u16* __restrict__ wg,
                                                   const u16* __restrict__ wu, const float* __restrict__ stats,
                                                   const float* __restrict__ cs, u16* __restrict__ gout) {
  extern __shared__ __attribute__((aligned(16))) char smem_[];
  char LAS* lds = (char LAS*)smem_;
  const int tid = threadIdx.x, lane = tid & 63;
  const int w = __builtin_amdgcn_readfirstlane(tid >> 6);
  const int bid = blockIdx.x, xcd = bid & 7, idx = bid >> 3;
  const int rt = (xcd >> 1) * 4 + (idx & 3), ct = (xcd & 1) * 8 + (idx >> 2);
  const int row0 = rt * 128, f0 = ct * 96;
  const int wr = w >> 1, wc = w & 1;
  const int g = lane >> 4, m = lane & 15;
  constexpr int NI = 5, NST = 3, NIT = 10;
  f32x4 st[2][4];
#pragma unroll
  for (int rb = 0; rb < 2; ++rb)
#pragma unroll
    for (int j = 0; j < 4; ++j)
      st[rb][j] = *(const f32x4*)(stats + ((size_t)(row0 + 32 * wr + 16 * rb + m) * 8 + 2 * j) * 2);
  f32x4 csg[3], csu[3];
#pragma unroll
  for (int p = 0; p < 3; ++p) {
    csg[p] = *(const f32x4*)(cs + f0 + 48 * wc + 16 * p + 4 * g);
    csu[p] = *(const f32x4*)(cs + F_ + f0 + 48 * wc + 16 * p + 4 * g);
  }
  const char* gp[NI];
#pragma unroll
  for (int i = 0; i < NI; ++i) {
    const int pi = i * 8 + w;
    if (i < 2) {
      const int row = 8 * pi + (lane >> 3);
      const int c = (lane & 7) ^ ((row >> 1) & 7);
      gp[i] = (const char*)(hh + (size_t)(row0 + row) * D_ + c * 8);
    } else {
      const int ci = (pi - 16) * 64 + lane;
      const int pr = ci / 24, pc = ci % 24;
      const int c = pc ^ (BMap<192>::fseg(pr) << 1);
      const u16* src = c < 12 ? wg + f0 + c * 8 : wu + f0 + (c - 12) * 8;
      gp[i] = (const char*)(src + (size_t)pr * F_);
    }
  }
  auto issue = [&](int st_) {
    char LAS* dst = lds + (st_ % NST) * K4_STAGE + w * 1024;
#pragma unroll
    for (int i = 0; i < NI; ++i) glds16(gp[i] + (size_t)st_ * (i < 2 ? 128 : 64 * F_ * 2), dst + i * 8192);
  };
#pragma unroll
  for (int s = 0; s < NST - 1; ++s) issue(s);
  int blo[6], bhi[6];
#pragma unroll
  for (int p = 0; p < 6; ++p) {
    const int cb = (p < 3 ? 0 : 6) + 3 * wc + (p % 3);
    blo[p] = boff_lo<192>(cb, lane);
    bhi[p] = boff_hi<192>(cb, lane);
  }
  f32x4 acc[2][6];
#pragma unroll
  for (int rb = 0; rb < 2; ++rb)
#pragma unroll
    for (int p = 0; p < 6; ++p) acc[rb][p] = f32x4{0.f, 0.f, 0.f, 0.f};
#pragma unroll
  for (int t = 0; t < NIT; ++t) {
    const int issued = (t + NST - 1 < NIT) ? (t + NST - 1) : NIT;
    wait_stage<NI>(issued - t - 1);
    BAR();
    if (t + NST - 1 < NIT) issue(t + NST - 1);
    const char LAS* sa = lds + (t % NST) * K4_STAGE;
    const char LAS* sb = sa + 16384;
#pragma unroll
    for (int kk = 0; kk < 2; ++kk) {
      const bf16x8 a0 = read_afrag(sa, 32 * wr + m, kk * 4 + g);
      const bf16x8 a1 = read_afrag(sa, 32 * wr + 16 + m, kk * 4 + g);
#pragma unroll
      for (int p = 0; p < 6; ++p) {
        const bf16x8 bfr = read_b2(sb + kk * 32 * 384 + blo[p], sb + kk * 32 * 384 + bhi[p]);
        acc[0][p] = MFMA16(bfr, a0, acc[0][p]);
        acc[1][p] = MFMA16(bfr, a1, acc[1][p]);
      }
    }
  }
#pragma unroll
  for (int rb = 0; rb < 2; ++rb) {
    const float s1 = (st[rb][0][0] + st[rb][0][2]) + (st[rb][1][0] + st[rb][1][2]) + (st[rb][2][0] + st[rb][2][2]) + (st[rb][3][0] + st[rb][3][2]);
    const float s2 = (st[rb][0][1] + st[rb][0][3]) + (st[rb][1][1] + st[rb][1][3]) + (st[rb][2][1] + st[rb][2][3]) + (st[rb][3][1] + st[rb][3][3]);
    const float mean = s1 * (1.0f / 640.0f);
    float var = s2 * (1.0f / 640.0f) - mean * mean;
    var = var < 0.f ? 0.f : var;
    const float rstd = 1.0f / sqrtf(var + LN_EPS);
    u16* dst = gout + (size_t)(row0 + 32 * wr + 16 * rb + m) * F_ + f0 + 48 * wc + 4 * g;
#pragma unroll
    for (int p = 0; p < 3; ++p) {
      float r4[4];
#pragma unroll
      for (int j = 0; j < 4; ++j) {
        const float ga = (acc[rb][p][j] - mean * csg[p][j]) * rstd;
        const float up = (acc[rb][p + 3][j] - mean * csu[p][j]) * rstd;
        r4[j] = gelu_tanh(ga) * up;
      }
      u32x2 o;
      o[0] = pk_bf16(r4[0], r4[1]);
      o[1] = pk_bf16(r4[2], r4[3]);
      *(u32x2*)(dst + 16 * p) = o;
    }
  }
}

extern "C" void kernel_launch(void* const* d_in, const int* in_sizes, int n_in, void* d_out, int out_size,
                              void* d_ws, size_t ws_size, hipStream_t stream) {
  const u16* x = (const u16*)d_in[0];
  const u16* wq = (const u16*)d_in[1];
  const u16* wk = (const u16*)d_in[2];
  const u16* wv = (const u16*)d_in[3];
  const u16* wo = (const u16*)d_in[4];
  const u16* wg = (const u16*)d_in[6];
  const u16* wu = (const u16*)d_in[7];
  const u16* wd = (const u16*)d_in[8];
  char* ws = (char*)d_ws;
  constexpr int K3_LDS = 4 * 36864;
  hipFuncSetAttribute((const void*)k1_qkv, hipFuncAttributeMaxDynamicSharedMemorySize, K1_LDS);
  hipFuncSetAttribute((const void*)k2_attn, hipFuncAttributeMaxDynamicSharedMemorySize, K2_LDS);
  hipFuncSetAttribute((const void*)k3_proj<640, true>, hipFuncAttributeMaxDynamicSharedMemorySize, K3_LDS);
  hipFuncSetAttribute((const void*)k3_proj<1536, false>, hipFuncAttributeMaxDynamicSharedMemorySize, K3_LDS);
  hipFuncSetAttribute((const void*)k4_ffn1, hipFuncAttributeMaxDynamicSharedMemorySize, K4_LDS);
  k1_qkv<<<256, 256, K1_LDS, stream>>>(x, wq, wk, wv, wg, wu, ws);
  k2_attn<<<256, 512, K2_LDS, stream>>>(ws, ws);
  k3_proj<640, true><<<256, 256, K3_LDS, stream>>>((const u16*)(ws + WS_O), wo, x, (u16*)(ws + WS_H),
                                                     (float*)(ws + WS_ST));
  k4_ffn1<<<256, 512, K4_LDS, stream>>>((const u16*)(ws + WS_H), wg, wu, (const float*)(ws + WS_ST),
                                         (const float*)(ws + WS_CS), (u16*)(ws + WS_G));
  k3_proj<1536, false><<<256, 256, K3_LDS, stream>>>((const u16*)(ws + WS_G), wd, (const u16*)(ws + WS_H),
                                                      (u16*)d_out, nullptr);
}
```

```cpp
#include <hip/hip_runtime.h>
#include <hip/hip_bf16.h>
#include <stdint.h>
#include <type_traits>

typedef __attribute__((ext_vector_type(8))) short bf16x8;
typedef __attribute__((ext_vector_type(4))) short s16x4;
typedef __attribute__((ext_vector_type(4))) float f32x4;
typedef __attribute__((ext_vector_type(2))) float f32x2;
typedef __attribute__((ext_vector_type(16))) float f32x16;
typedef __attribute__((ext_vector_type(2))) __bf16 bf16x2_t;
typedef __attribute__((ext_vector_type(4))) unsigned u32x4;
typedef __attribute__((ext_vector_type(2))) unsigned u32x2;
typedef unsigned short u16;
#define LAS __attribute__((address_space(3)))
#define DI __device__ __forceinline__

constexpr int S_ = 2048, D_ = 640, F_ = 1536;
constexpr float QSCALE = 0.22360679774997896f * 1.4426950408889634f;
constexpr float LN_EPS = 1e-5f;
constexpr float FAST_MAX = 40.0f;

constexpr size_t WS_Q = 0;
constexpr size_t WS_KF = WS_Q + 2048 * 640 * 2;
constexpr size_t WS_VF = WS_KF + 64 * 2048;
constexpr size_t WS_KMX = WS_VF + 64 * 2048;
constexpr size_t WS_CS = WS_KMX + 256;
constexpr size_t WS_O = WS_CS + 3072 * 4;
constexpr size_t WS_H = WS_O + 2048 * 640 * 2;
constexpr size_t WS_ST = WS_H + 2048 * 640 * 2;
constexpr size_t WS_G = WS_ST + 2048 * 16 * 4;

#define STAMP(k, i)
DI unsigned pk_bf16(float a, float b) {
  f32x2 v = {a, b};
  return __builtin_bit_cast(unsigned, __builtin_convertvector(v, bf16x2_t));
}
DI float bf_lo(unsigned u) { return __uint_as_float(u << 16); }
DI float bf_hi(unsigned u) { return __uint_as_float(u & 0xffff0000u); }
DI void glds16(const void* g, char LAS* l) {
  __builtin_amdgcn_global_load_lds((const unsigned*)g, (unsigned LAS*)l, 16, 0, 0);
}
DI s16x4 tr_read(const char LAS* p) { return __builtin_amdgcn_ds_read_tr16_b64_v4i16((s16x4 LAS*)p); }
#define MFMA16(a, b, c) __builtin_amdgcn_mfma_f32_16x16x32_bf16((a), (b), (c), 0, 0, 0)
#define MFMA32(a, b, c) __builtin_amdgcn_mfma_f32_32x32x16_bf16((a), (b), (c), 0, 0, 0)
#define WAIT_V(n) asm volatile("s_waitcnt vmcnt(%0)" ::"i"(n) : "memory")
#define BAR() __builtin_amdgcn_s_barrier()

template <int BN> struct BMap;
template <> struct BMap<80> {
  static DI int prow(int k) { return k ^ (((k >> 3) & 1) << 2); }
  static DI int fseg(int k) { return 0; }
};
template <> struct BMap<96> {
  static DI int prow(int k) { return k; }
  static DI int fseg(int k) { return (k >> 3) & 1; }
};
template <> struct BMap<192> {
  static DI int prow(int k) { return k; }
  static DI int fseg(int k) { return ((k >> 1) & 1) | (((k >> 3) & 1) << 1); }
};

template <int BN> DI int boff_lo(int cb, int lane) {
  const int g = lane >> 4, q = (lane >> 2) & 3, p = lane & 3, k = 8 * g + q;
  return BMap<BN>::prow(k) * (2 * BN) + ((cb ^ BMap<BN>::fseg(k)) * 32) + p * 8;
}
template <int BN> DI int boff_hi(int cb, int lane) {
  const int g = lane >> 4, q = (lane >> 2) & 3, p = lane & 3, k = 8 * g + q + 4;
  return BMap<BN>::prow(k) * (2 * BN) + ((cb ^ BMap<BN>::fseg(k)) * 32) + p * 8;
}
DI bf16x8 read_b2(const char LAS* plo, const char LAS* phi) {
  s16x4 lo = tr_read(plo), hi = tr_read(phi);
  return __builtin_shufflevector(lo, hi, 0, 1, 2, 3, 4, 5, 6, 7);
}
DI bf16x8 read_afrag(const char LAS* img, int row, int c) {
  return *(const bf16x8 LAS*)(img + row * 128 + ((c ^ ((row >> 1) & 7)) * 16));
}
template <int I, int N, class F> DI void sfor(F&& f) {
  if constexpr (I < N) {
    f(std::integral_constant<int, I>{});
    sfor<I + 1, N>(f);
  }
}
template <int OFF> DI void a_tr(s16x4& d, unsigned addr) {
  asm volatile("ds_read_b64_tr_b16 %0, %1 offset:%2" : "=v"(d) : "v"(addr), "i"(OFF));
}
template <int OFF> DI void a_b128(bf16x8& d, unsigned addr) {
  asm volatile("ds_read_b128 %0, %1 offset:%2" : "=v"(d) : "v"(addr), "i"(OFF));
}
template <int N> DI void lgkm_wait() { asm volatile("s_waitcnt lgkmcnt(%0)" ::"i"(N) : "memory"); }
DI void pin(bf16x8& a) { asm volatile("" : "+v"(a)); }
DI void pin(s16x4& a) { asm volatile("" : "+v"(a)); }
DI bf16x8 cat(const s16x4& lo, const s16x4& hi) { return __builtin_shufflevector(lo, hi, 0, 1, 2, 3, 4, 5, 6, 7); }
DI unsigned ldsaddr(char LAS* p) { return (unsigned)(size_t)p; }
DI bf16x8 ones8() {
  bf16x8 o;
#pragma unroll
  for (int j = 0; j < 8; ++j) o[j] = 0x3F80;
  return o;
}
template <int N> DI void wait_stage(int pend) {
  if (pend >= 3) WAIT_V(3 * N);
  else if (pend == 2) WAIT_V(2 * N);
  else if (pend == 1) WAIT_V(N);
  else WAIT_V(0);
}

template <int BN, int NST, int NIT, bool STATS>
struct SmallGemm {
  static constexpr int CPR = BN / 8;
  static constexpr int BPIECES = 128 * BN * 2 / 1024;
  static constexpr int NI = (16 + BPIECES) / 4;
  static constexpr int STAGE = (16 + BPIECES) * 1024;
  static constexpr int NCB = BN / 16;

  static DI void run(char LAS* lds, const u16* A, int lda, int row0, const u16* B, int ldb, int n0, int nmax,
                     int rot, f32x4 (&acc)[NCB], f32x4& rs, f32x4& sq, f32x4& cs0, f32x4& cs1) {
    const int tid = threadIdx.x, lane = tid & 63;
    const int w = __builtin_amdgcn_readfirstlane(tid >> 6);
    const char* gp[NI];
#pragma unroll
    for (int i = 0; i < NI; ++i) {
      const int pi = i * 4 + w;
      if (i < 4) {
        const int sub = pi >> 3, row = 8 * (pi & 7) + (lane >> 3);
        const int c = (lane & 7) ^ ((row >> 1) & 7);
        gp[i] = (const char*)(A + (size_t)(row0 + row) * lda + sub * 64 + c * 8);
      } else {
        const int ci = (pi - 16) * 64 + lane;
        const int pr = ci / CPR, pc = ci % CPR;
        const int k = BMap<BN>::prow(pr);
        const int c = pc ^ (BMap<BN>::fseg(k) << 1);
        int n = n0 + c * 8;
        n = n > nmax - 8 ? nmax - 8 : n;
        gp[i] = (const char*)(B + (size_t)k * ldb + n);
      }
    }
    const int bstep = 128 * ldb * 2;
    auto issue = [&](int st) {
      char LAS* dst = lds + (st % NST) * STAGE + w * 1024;
#pragma unroll
      for (int i = 0; i < NI; ++i)
        glds16(gp[i] + (size_t)st * (i < 4 ? 256 : bstep), dst + i * 4096);
    };
#pragma unroll
    for (int s = 0; s < NST - 1; ++s) issue(s);
    const int arow = 16 * w + (lane & 15), g = lane >> 4;
    unsigned blo[NCB], bhi[NCB];
#pragma unroll
    for (int p = 0; p < NCB; ++p) {
      int cb = p + rot;
      cb = cb >= NCB ? cb - NCB : cb;
      blo[p] = ldsaddr(lds) + 16384 + boff_lo<BN>(cb, lane);
      bhi[p] = ldsaddr(lds) + 16384 + boff_hi<BN>(cb, lane);
    }
    const unsigned aa0 = ldsaddr(lds) + arow * 128 + ((g ^ ((arow >> 1) & 7)) * 16);
    const unsigned aa1 = ldsaddr(lds) + arow * 128 + (((4 + g) ^ ((arow >> 1) & 7)) * 16);
    const bf16x8 ones = ones8();
    bf16x8 af[2];
    s16x4 bl[2][NCB], bh[2][NCB];
    sfor<0, NIT>([&](auto tc) {
      constexpr int t = decltype(tc)::value;
      constexpr int issued = (t + NST - 1 < NIT) ? (t + NST - 1) : NIT;
      wait_stage<NI>(issued - t - 1);
      BAR();
      if constexpr (t + NST - 1 < NIT) issue(t + NST - 1);
      const unsigned so = (t % NST) * STAGE;
      auto rd = [&](auto kc) {
        constexpr int kk = decltype(kc)::value;
        a_b128<(kk >> 1) * 8192>(af[kk & 1], ((kk & 1) ? aa1 : aa0) + so);
#pragma unroll
        for (int p = 0; p < NCB; ++p) {
          a_tr<kk * 32 * 2 * BN>(bl[kk & 1][p], blo[p] + so);
          a_tr<kk * 32 * 2 * BN>(bh[kk & 1][p], bhi[p] + so);
        }
      };
      rd(std::integral_constant<int, 0>{});
      sfor<0, 4>([&](auto kc) {
        constexpr int kk = decltype(kc)::value;
        if constexpr (kk < 3) {
          rd(std::integral_constant<int, kk + 1>{});
          lgkm_wait<1 + 2 * NCB>();
        } else {
          lgkm_wait<0>();
        }
        pin(af[kk & 1]);
#pragma unroll
        for (int p = 0; p < NCB; ++p) { pin(bl[kk & 1][p]); pin(bh[kk & 1][p]); }
        if (STATS) {
          rs = MFMA16(ones, af[kk & 1], rs);
          sq = MFMA16(af[kk & 1], af[kk & 1], sq);
        }
#pragma unroll
        for (int p = 0; p < NCB; ++p) {
          const bf16x8 bfr = cat(bl[kk & 1][p], bh[kk & 1][p]);
          acc[p] = MFMA16(bfr, af[kk & 1], acc[p]);
          if (STATS && p == 0) cs0 = MFMA16(bfr, ones, cs0);
          if (STATS && p == 4) cs1 = MFMA16(bfr, ones, cs1);
        }
        __builtin_amdgcn_sched_barrier(0);
      });
    });
  }
};

DI void finish_stats(const f32x4& rs, const f32x4& sq, int lane, float& mean, float& rstd) {
  const int m = lane & 15;
  const int r = m & 3;
  float d = r == 0 ? sq[0] : (r == 1 ? sq[1] : (r == 2 ? sq[2] : sq[3]));
  d = __shfl(d, (m >> 2) * 16 + m);
  mean = rs[0] * (1.0f / 640.0f);
  float var = d * (1.0f / 640.0f) - mean * mean;
  var = var < 0.f ? 0.f : var;
  rstd = 1.0f / sqrtf(var + LN_EPS);
}

constexpr int K1_LDS = 3 * 40960 + 1024;
__global__ __launch_bounds__(256, 1) void k1_qkv(const u16* __restrict__ x, const u16* __restrict__ wq,
                                                  const u16* __restrict__ wk, const u16* __restrict__ wv,
                                                  const u16* __restrict__ wg, const u16* __restrict__ wu,
                                                  char* __restrict__ ws) {
  extern __shared__ __attribute__((aligned(16))) char smem_[];
  char LAS* lds = (char LAS*)smem_;
  const int tid = threadIdx.x, lane = tid & 63;
  STAMP(0, 0);
  const int w = __builtin_amdgcn_readfirstlane(tid >> 6);
  const int bid = blockIdx.x, xcd = bid & 7, idx = bid >> 3;
  const int rt = xcd * 4 + (idx & 3), ct = idx >> 2;
  const int row0 = rt * 64;
  const int g = lane >> 4, m = lane & 15;
  f32x4 z4 = {0.f, 0.f, 0.f, 0.f};
  f32x4 rs = z4, sq = z4;
  float LAS* cs_l = (float LAS*)(lds + 3 * 40960);
  if (ct < 7) {
    using G = SmallGemm<96, 3, 5, true>;
    f32x4 acc[6], cs0 = z4, cs1 = z4;
#pragma unroll
    for (int p = 0; p < 6; ++p) acc[p] = z4;
    G::run(lds, x, D_, row0, wq, D_, ct * 96, D_, w, acc, rs, sq, cs0, cs1);
    float mean, rstd;
    finish_stats(rs, sq, lane, mean, rstd);
    if (m == 0) {
      *(f32x4 LAS*)(cs_l + w * 16 + 4 * g) = cs0;
      if (w < 2) *(f32x4 LAS*)(cs_l + (4 + w) * 16 + 4 * g) = cs1;
    }
    __syncthreads();
    u16* q = (u16*)(ws + WS_Q);
    const float sc = rstd * QSCALE;
#pragma unroll
    for (int p = 0; p < 6; ++p) {
      int cb = p + w;
      cb = cb >= 6 ? cb - 6 : cb;
      const int n = ct * 96 + cb * 16 + 4 * g;
      const f32x4 c = *(const f32x4 LAS*)(cs_l + cb * 16 + 4 * g);
      if (n < D_) {
        u32x2 o;
        o[0] = pk_bf16((acc[p][0] - mean * c[0]) * sc, (acc[p][1] - mean * c[1]) * sc);
        o[1] = pk_bf16((acc[p][2] - mean * c[2]) * sc, (acc[p][3] - mean * c[3]) * sc);
        *(u32x2*)(q + (size_t)(row0 + 16 * w + m) * D_ + n) = o;
      }
    }
  } else {
    constexpr int NI = 7, STAGE = 28 * 1024, NST = 4, NIT = 5;
    const char* gp[NI];
#pragma unroll
    for (int i = 0; i < NI; ++i) {
      const int pi = i * 4 + w;
      if (i < 4) {
        const int sub = pi >> 3, row = 8 * (pi & 7) + (lane >> 3);
        const int c = (lane & 7) ^ ((row >> 1) & 7);
        gp[i] = (const char*)(x + (size_t)(row0 + row) * D_ + sub * 64 + c * 8);
      } else {
        int pj = pi - 16;
        pj = pj >= 10 ? pj - 10 : pj;
        const u16* src = pj < 5 ? wk : wv;
        gp[i] = (const char*)src + (pj < 5 ? pj : pj - 5) * 1024 + lane * 16;
      }
    }
    auto issue = [&](int st) {
      char LAS* dst = lds + (st % NST) * STAGE + w * 1024;
#pragma unroll
      for (int i = 0; i < NI; ++i) glds16(gp[i] + (size_t)st * (i < 4 ? 256 : 5120), dst + i * 4096);
    };
#pragma unroll
    for (int s = 0; s < NST - 1; ++s) issue(s);
    const int arow = 16 * w + m;
    const int q4 = (lane >> 2) & 3, p4 = lane & 3;
    const int bo = (8 * g + q4) * 40 + p4 * 8;
    f32x4 ak0 = z4, ak1 = z4, av0 = z4, av1 = z4, ck0 = z4, ck1 = z4, cv0 = z4, cv1 = z4;
    const bf16x8 ones = ones8();
    const unsigned aa0 = ldsaddr(lds) + arow * 128 + ((g ^ ((arow >> 1) & 7)) * 16);
    const unsigned aa1 = ldsaddr(lds) + arow * 128 + (((4 + g) ^ ((arow >> 1) & 7)) * 16);
    const unsigned bb = ldsaddr(lds) + 16384 + bo;
    sfor<0, NIT>([&](auto tc) {
      constexpr int t = decltype(tc)::value;
      constexpr int issued = (t + NST - 1 < NIT) ? (t + NST - 1) : NIT;
      wait_stage<NI>(issued - t - 1);
      BAR();
      if constexpr (t + NST - 1 < NIT) issue(t + NST - 1);
      const unsigned so = (t % NST) * STAGE;
      sfor<0, 4>([&](auto kc) {
        constexpr int kk = decltype(kc)::value;
        bf16x8 af;
        s16x4 r[8];
        a_b128<(kk >> 1) * 8192>(af, ((kk & 1) ? aa1 : aa0) + so);
        constexpr int o = kk * 32 * 40;
        a_tr<o>(r[0], bb + so); a_tr<o + 160>(r[1], bb + so); a_tr<o + 32>(r[2], bb + so); a_tr<o + 192>(r[3], bb + so);
        a_tr<5120 + o>(r[4], bb + so); a_tr<5120 + o + 160>(r[5], bb + so);
        a_tr<5120 + o + 32>(r[6], bb + so); a_tr<5120 + o + 192>(r[7], bb + so);
        lgkm_wait<0>();
        pin(af);
#pragma unroll
        for (int j = 0; j < 8; ++j) pin(r[j]);
        const bf16x8 k0 = cat(r[0], r[1]), k1 = cat(r[2], r[3]), v0 = cat(r[4], r[5]), v1 = cat(r[6], r[7]);
        rs = MFMA16(ones, af, rs);
        sq = MFMA16(af, af, sq);
        ak0 = MFMA16(k0, af, ak0);
        ak1 = MFMA16(k1, af, ak1);
        ck0 = MFMA16(k0, ones, ck0);
        ck1 = MFMA16(k1, ones, ck1);
        av0 = MFMA16(af, v0, av0);
        av1 = MFMA16(af, v1, av1);
        cv0 = MFMA16(ones, v0, cv0);
        cv1 = MFMA16(ones, v1, cv1);
      });
    });
    float mean, rstd;
    finish_stats(rs, sq, lane, mean, rstd);
    u16* kf = (u16*)(ws + WS_KF);
    u16* vf = (u16*)(ws + WS_VF);
    const int key = row0 + 16 * w + m;
    const int blk = key >> 5, kk5 = key & 31;
    const int r = (kk5 & 19) | ((kk5 & 4) << 1) | ((kk5 & 8) >> 1);
    float kv[4], n2;
    u32x2 o;
#pragma unroll
    for (int j = 0; j < 4; ++j) kv[j] = (ak0[j] - mean * ck0[j]) * rstd;
    o[0] = pk_bf16(kv[0], kv[1]);
    o[1] = pk_bf16(kv[2], kv[3]);
    n2 = bf_lo(o[0]) * bf_lo(o[0]) + bf_hi(o[0]) * bf_hi(o[0]) + bf_lo(o[1]) * bf_lo(o[1]) + bf_hi(o[1]) * bf_hi(o[1]);
    *(u32x2*)(kf + (size_t)blk * 1024 + ((g >> 1) * 32 + r) * 8 + (g & 1) * 4) = o;
    {
      u32x2 o1;
#pragma unroll
      for (int j = 0; j < 4; ++j) kv[j] = (ak1[j] - mean * ck1[j]) * rstd;
      o1[0] = pk_bf16(kv[0], kv[1]);
      o1[1] = pk_bf16(kv[2], kv[3]);
      if (g == 0)
        n2 += bf_lo(o1[0]) * bf_lo(o1[0]) + bf_hi(o1[0]) * bf_hi(o1[0]) + bf_lo(o1[1]) * bf_lo(o1[1]) +
              bf_hi(o1[1]) * bf_hi(o1[1]);
      if (g == 1) { o1[0] = 0x3F803F80u; o1[1] = 0x00003F80u; }
      if (g >= 2) { o1[0] = 0u; o1[1] = 0u; }
      *(u32x2*)(kf + (size_t)blk * 1024 + 512 + ((g >> 1) * 32 + r) * 8 + (g & 1) * 4) = o1;
    }
    n2 += __shfl_xor(n2, 16);
    n2 += __shfl_xor(n2, 32);
#pragma unroll
    for (int s = 1; s < 16; s <<= 1) n2 = fmaxf(n2, __shfl_xor(n2, s));
    float LAS* red = (float LAS*)(lds + 4 * 28 * 1024);
    if (lane == 0) red[w] = n2;
    {
      float mj[4], rj[4];
#pragma unroll
      for (int j = 0; j < 4; ++j) {
        mj[j] = __shfl(mean, 4 * g + j);
        rj[j] = __shfl(rstd, 4 * g + j);
      }
      const int key0 = row0 + 16 * w + 4 * g;
      const int vb = key0 >> 5, k5 = key0 & 31;
      const int s = k5 >> 4, h = (k5 >> 3) & 1, j0 = k5 & 7;
      u16* base = vf + (size_t)vb * 1024 + s * 512 + h * 256 + j0;
      u32x2 ov;
      ov[0] = pk_bf16((av0[0] - mj[0] * cv0[0]) * rj[0], (av0[1] - mj[1] * cv0[1]) * rj[1]);
      ov[1] = pk_bf16((av0[2] - mj[2] * cv0[2]) * rj[2], (av0[3] - mj[3] * cv0[3]) * rj[3]);
      *(u32x2*)(base + m * 8) = ov;
      ov[0] = pk_bf16((av1[0] - mj[0] * cv1[0]) * rj[0], (av1[1] - mj[1] * cv1[1]) * rj[1]);
      ov[1] = pk_bf16((av1[2] - mj[2] * cv1[2]) * rj[2], (av1[3] - mj[3] * cv1[3]) * rj[3]);
      if (m == 4) { ov[0] = 0x3F803F80u; ov[1] = 0x3F803F80u; }
      if (m > 4) { ov[0] = 0u; ov[1] = 0u; }
      *(u32x2*)(base + (16 + m) * 8) = ov;
    }
    __syncthreads();
    if (tid == 0) ((float*)(ws + WS_KMX))[rt] = fmaxf(fmaxf(red[0], red[1]), fmaxf(red[2], red[3]));
    {
      const int c0 = rt * 96;
      const u16* src = c0 < F_ ? wg + c0 : wu + (c0 - F_);
      float a[8];
#pragma unroll
      for (int j = 0; j < 8; ++j) a[j] = 0.f;
      const int ch = tid % 12, rg = tid / 12;
      if (rg < 20) {
#pragma unroll 8
        for (int i = 0; i < 32; ++i) {
          const u32x4 v = *(const u32x4*)(src + (size_t)(rg * 32 + i) * F_ + ch * 8);
#pragma unroll
          for (int e = 0; e < 4; ++e) {
            a[2 * e] += bf_lo(v[e]);
            a[2 * e + 1] += bf_hi(v[e]);
          }
        }
      }
      float LAS* cred = (float LAS*)lds;
      if (rg < 20) {
#pragma unroll
        for (int j = 0; j < 8; ++j) cred[rg * 96 + ch * 8 + j] = a[j];
      }
      __syncthreads();
      if (tid < 96) {
        float s = 0.f;
#pragma unroll
        for (int i = 0; i < 20; ++i) s += cred[i * 96 + tid];
        ((float*)(ws + WS_CS))[c0 + tid] = s;
      }
    }
  }
  STAMP(0, 1);
}

constexpr int K2_LDS = 4 * 256 * 96 + 4096;
DI void split3(float M, unsigned& s01, unsigned& s2) {
  const float n = -M;
  const unsigned hi = __float_as_uint(n) & 0xffff0000u;
  const float r1 = n - __uint_as_float(hi);
  const unsigned mid = __float_as_uint(r1) & 0xffff0000u;
  const float r2 = r1 - __uint_as_float(mid);
  const unsigned lo = __float_as_uint(r2) & 0xffff0000u;
  s01 = (hi >> 16) | mid;
  s2 = lo >> 16;
}
__global__ __launch_bounds__(512, 2) void k2_attn(const char* __restrict__ wsr, char* __restrict__ ws) {
  extern __shared__ __attribute__((aligned(16))) char smem_[];
  char LAS* lds = (char LAS*)smem_;
  const int tid = threadIdx.x, lane = tid & 63;
  STAMP(1, 0);
  const int w = __builtin_amdgcn_readfirstlane(tid >> 6);
  const int ch = w & 1, ks = w >> 1;
  const int r = lane & 31, h = lane >> 5;
  const int qrow0 = blockIdx.x * 8;
  const u16* q = (const u16*)(wsr + WS_Q);
  const bf16x8* kf = (const bf16x8*)(wsr + WS_KF) + (size_t)ks * 16 * 128 + lane;
  const bf16x8* vf = (const bf16x8*)(wsr + WS_VF) + (size_t)ks * 16 * 128 + lane;
  bf16x8 q0[4], q1[4];
  float qn[4];
#pragma unroll
  for (int gq = 0; gq < 4; ++gq) {
    const u16* src = q + (size_t)(qrow0 + ch * 4 + gq) * D_ + r * 20;
    const u32x2 a = *(const u32x2*)(src + 8 * h), b = *(const u32x2*)(src + 8 * h + 4);
    u32x2 c = *(const u32x2*)(src + 16);
    if (h) { c[0] = 0u; c[1] = 0u; }
    u32x4 f0 = {a[0], a[1], b[0], b[1]};
    u32x4 f1 = {c[0], c[1], 0u, 0u};
    float n2 = 0.f;
#pragma unroll
    for (int e = 0; e < 4; ++e) n2 += bf_lo(f0[e]) * bf_lo(f0[e]) + bf_hi(f0[e]) * bf_hi(f0[e]);
#pragma unroll
    for (int e = 0; e < 2; ++e) n2 += bf_lo(f1[e]) * bf_lo(f1[e]) + bf_hi(f1[e]) * bf_hi(f1[e]);
    n2 += __shfl_xor(n2, 32);
    qn[gq] = n2;
    q0[gq] = __builtin_bit_cast(bf16x8, f0);
    q1[gq] = __builtin_bit_cast(bf16x8, f1);
  }
  float kmx = ((const float*)(wsr + WS_KMX))[r];
#pragma unroll
  for (int s = 1; s < 32; s <<= 1) kmx = fmaxf(kmx, __shfl_xor(kmx, s));
  float M[4];
  bool unsafe = false;
#pragma unroll
  for (int gq = 0; gq < 4; ++gq) {
    M[gq] = sqrtf(qn[gq] * kmx) * 1.001f + 1e-3f;
    unsafe |= !(M[gq] <= FAST_MAX);
  }
  f32x16 z16;
#pragma unroll
  for (int i = 0; i < 16; ++i) z16[i] = 0.f;
  if (__syncthreads_or(unsafe)) {
    float mx[4] = {-INFINITY, -INFINITY, -INFINITY, -INFINITY};
    for (int b = 0; b < 16; ++b) {
      const bf16x8 k0 = kf[b * 128], k1 = kf[b * 128 + 64];
#pragma unroll
      for (int gq = 0; gq < 4; ++gq) {
        f32x16 s = MFMA32(k0, q0[gq], z16);
        s = MFMA32(k1, q1[gq], s);
#pragma unroll
        for (int i = 0; i < 16; ++i) mx[gq] = fmaxf(mx[gq], s[i]);
      }
    }
    float LAS* mred = (float LAS*)lds;
#pragma unroll
    for (int gq = 0; gq < 4; ++gq) {
      mx[gq] = fmaxf(mx[gq], __shfl_xor(mx[gq], 32));
      if (h == 0) mred[ks * 256 + ch * 128 + gq * 32 + r] = mx[gq];
    }
    __syncthreads();
#pragma unroll
    for (int gq = 0; gq < 4; ++gq) {
      const int c = ch * 128 + gq * 32 + r;
      M[gq] = fmaxf(fmaxf(mred[c], mred[256 + c]), fmaxf(mred[512 + c], mred[768 + c]));
    }
    __syncthreads();
  }
#pragma unroll
  for (int gq = 0; gq < 4; ++gq) {
    unsigned s01, s2;
    split3(M[gq], s01, s2);
    u32x4 f1 = __builtin_bit_cast(u32x4, q1[gq]);
    if (h == 0) { f1[2] = s01; f1[3] = s2; }
    q1[gq] = __builtin_bit_cast(bf16x8, f1);
  }
  STAMP(1, 2);
  f32x16 o[4];
#pragma unroll
  for (int gq = 0; gq < 4; ++gq) o[gq] = z16;
  {
    bf16x8 kA0, kA1, vA0, vA1, kB0, kB1, vB0, vB1;
    f32x16 sA, sB;
    auto ld = [&](bf16x8& a0, bf16x8& a1, bf16x8& b0, bf16x8& b1, int b) {
      const bf16x8* pk = kf + b * 128;
      const bf16x8* pv = vf + b * 128;
      asm volatile("global_load_dwordx4 %0, %1, off" : "=v"(a0) : "v"(pk));
      asm volatile("global_load_dwordx4 %0, %1, off offset:1024" : "=v"(a1) : "v"(pk));
      asm volatile("global_load_dwordx4 %0, %1, off" : "=v"(b0) : "v"(pv));
      asm volatile("global_load_dwordx4 %0, %1, off offset:1024" : "=v"(b1) : "v"(pv));
    };
    auto qk = [&](f32x16& sn, const bf16x8& a0, const bf16x8& a1, int gq) {
      sn = MFMA32(a0, q0[gq], z16);
      sn = MFMA32(a1, q1[gq], sn);
    };
    auto sm_pv = [&](const f32x16& sc, const bf16x8& b0, const bf16x8& b1, int gq) {
      u32x4 p0, p1;
#pragma unroll
      for (int i = 0; i < 4; ++i)
        p0[i] = pk_bf16(__builtin_amdgcn_exp2f(sc[2 * i]), __builtin_amdgcn_exp2f(sc[2 * i + 1]));
      o[gq] = MFMA32(b0, __builtin_bit_cast(bf16x8, p0), o[gq]);
#pragma unroll
      for (int i = 0; i < 4; ++i)
        p1[i] = pk_bf16(__builtin_amdgcn_exp2f(sc[8 + 2 * i]), __builtin_amdgcn_exp2f(sc[8 + 2 * i + 1]));
      o[gq] = MFMA32(b1, __builtin_bit_cast(bf16x8, p1), o[gq]);
    };
#define SB() __builtin_amdgcn_sched_barrier(0)
#define VW2(n, a, b) asm volatile("s_waitcnt vmcnt(" #n ")" : "+v"(a), "+v"(b))
#define BLOCK(ck0, ck1, cv0, cv1, nk0, nk1, nv0, nv1, bnext)        \
    ld(nk0, nk1, nv0, nv1, bnext);                                    \
    qk(sB, ck0, ck1, 1); SB();                                        \
    VW2(4, cv0, cv1);                                                 \
    sm_pv(sA, cv0, cv1, 0); SB();                                     \
    qk(sA, ck0, ck1, 2); SB();                                        \
    sm_pv(sB, cv0, cv1, 1); SB();                                     \
    qk(sB, ck0, ck1, 3); SB();                                        \
    sm_pv(sA, cv0, cv1, 2); SB();                                     \
    VW2(2, nk0, nk1);                                                 \
    qk(sA, nk0, nk1, 0); SB();                                        \
    sm_pv(sB, cv0, cv1, 3); SB();
    ld(kA0, kA1, vA0, vA1, 0);
    VW2(2, kA0, kA1);
    qk(sA, kA0, kA1, 0);
    for (int b = 0; b < 16; b += 2) {
      BLOCK(kA0, kA1, vA0, vA1, kB0, kB1, vB0, vB1, b + 1)
      BLOCK(kB0, kB1, vB0, vB1, kA0, kA1, vA0, vA1, (b + 2 < 16 ? b + 2 : 15))
    }
    asm volatile("s_waitcnt vmcnt(0)" ::: "memory");
  }
  STAMP(1, 3);
  float LAS* red = (float LAS*)lds;
#pragma unroll
  for (int gq = 0; gq < 4; ++gq) {
    float LAS* dst = red + ((size_t)ks * 256 + ch * 128 + gq * 32 + r) * 24 + 4 * h;
#pragma unroll
    for (int t = 0; t < 3; ++t) {
      f32x4 v = {o[gq][4 * t], o[gq][4 * t + 1], o[gq][4 * t + 2], o[gq][4 * t + 3]};
      *(f32x4 LAS*)(dst + 8 * t) = v;
    }
  }
  __syncthreads();
  u16* og = (u16*)(ws + WS_O) + (size_t)qrow0 * D_;
  for (int it = tid; it < 1280; it += 512) {
    const int c = it / 5, qd = it % 5;
    f32x4 a = {0.f, 0.f, 0.f, 0.f};
    float l = 0.f;
#pragma unroll
    for (int s = 0; s < 4; ++s) {
      const float LAS* src = red + ((size_t)s * 256 + c) * 24;
      a += *(const f32x4 LAS*)(src + 4 * qd);
      l += src[20];
    }
    const float inv = 1.0f / l;
    u32x2 ov;
    ov[0] = pk_bf16(a[0] * inv, a[1] * inv);
    ov[1] = pk_bf16(a[2] * inv, a[3] * inv);
    *(u32x2*)(og + (size_t)it * 4) = ov;
  }
  STAMP(1, 1);
}

template <int KDIM, bool STATS_OUT>
__global__ __launch_bounds__(256, 1) void k3_proj(const u16* __restrict__ A, const u16* __restrict__ W,
                                                   const u16* __restrict__ res, u16* __restrict__ out,
                                                   float* __restrict__ stats) {
  extern __shared__ __attribute__((aligned(16))) char smem_[];
  char LAS* lds = (char LAS*)smem_;
  const int tid = threadIdx.x, lane = tid & 63;
  STAMP(STATS_OUT ? 2 : 4, 0);
  const int w = __builtin_amdgcn_readfirstlane(tid >> 6);
  const int bid = blockIdx.x, xcd = bid & 7, idx = bid >> 3;
  const int rt = xcd * 4 + (idx & 3), ct = idx >> 2;
  const int g = lane >> 4, m = lane & 15;
  const int row = rt * 64 + 16 * w + m;
  u32x2 rv[5];
#pragma unroll
  for (int p = 0; p < 5; ++p) rv[p] = *(const u32x2*)(res + (size_t)row * D_ + ct * 80 + p * 16 + 4 * g);
  using G = SmallGemm<80, 4, KDIM / 128, false>;
  f32x4 z4 = {0.f, 0.f, 0.f, 0.f};
  f32x4 acc[5], d0 = z4, d1 = z4, d2 = z4, d3 = z4;
#pragma unroll
  for (int p = 0; p < 5; ++p) acc[p] = z4;
  G::run(lds, A, KDIM, rt * 64, W, D_, ct * 80, D_, 0, acc, d0, d1, d2, d3);
  float s1 = 0.f, s2 = 0.f;
#pragma unroll
  for (int p = 0; p < 5; ++p) {
    u32x2 o;
    o[0] = pk_bf16(bf_lo(rv[p][0]) + acc[p][0], bf_hi(rv[p][0]) + acc[p][1]);
    o[1] = pk_bf16(bf_lo(rv[p][1]) + acc[p][2], bf_hi(rv[p][1]) + acc[p][3]);
    *(u32x2*)(out + (size_t)row * D_ + ct * 80 + p * 16 + 4 * g) = o;
    if (STATS_OUT) {
      const float a = bf_lo(o[0]), b = bf_hi(o[0]), c = bf_lo(o[1]), d = bf_hi(o[1]);
      s1 += (a + b) + (c + d);
      s2 += (a * a + b * b) + (c * c + d * d);
    }
  }
  if (STATS_OUT) {
    s1 += __shfl_xor(s1, 16); s1 += __shfl_xor(s1, 32);
    s2 += __shfl_xor(s2, 16); s2 += __shfl_xor(s2, 32);
    if (g == 0) { f32x2 v = {s1, s2}; *(f32x2*)(stats + ((size_t)row * 8 + ct) * 2) = v; }
  }
  STAMP(STATS_OUT ? 2 : 4, 1);
}

constexpr int K4_STAGE = 40960, K4_LDS = 3 * K4_STAGE;
DI float gelu_tanh(float x) {
  const float u = 0.7978845608028654f * (x + 0.044715f * x * x * x);
  const float e = __builtin_amdgcn_exp2f(u * 2.8853900817779268f);
  const float t = 1.0f - 2.0f * __builtin_amdgcn_rcpf(e + 1.0f);
  return 0.5f * x * (1.0f + t);
}
__global__ __launch_bounds__(512, 2) void k4_ffn1(const u16* __restrict__ hh, const u16* __restrict__ wg,
                                                   const u16* __restrict__ wu, const float* __restrict__ stats,
                                                   const float* __restrict__ cs, u16* __restrict__ gout) {
  extern __shared__ __attribute__((aligned(16))) char smem_[];
  char LAS* lds = (char LAS*)smem_;
  const int tid = threadIdx.x, lane = tid & 63;
  STAMP(3, 0);
  const int w = __builtin_amdgcn_readfirstlane(tid >> 6);
  const int bid = blockIdx.x, xcd = bid & 7, idx = bid >> 3;
  const int rt = (xcd >> 1) * 4 + (idx & 3), ct = (xcd & 1) * 8 + (idx >> 2);
  const int row0 = rt * 128, f0 = ct * 96;
  const int wr = w >> 1, wc = w & 1;
  const int g = lane >> 4, m = lane & 15;
  constexpr int NI = 5, NST = 3, NIT = 10;
  f32x4 st[2][4];
#pragma unroll
  for (int rb = 0; rb < 2; ++rb)
#pragma unroll
    for (int j = 0; j < 4; ++j)
      st[rb][j] = *(const f32x4*)(stats + ((size_t)(row0 + 32 * wr + 16 * rb + m) * 8 + 2 * j) * 2);
  f32x4 csg[3], csu[3];
#pragma unroll
  for (int p = 0; p < 3; ++p) {
    csg[p] = *(const f32x4*)(cs + f0 + 48 * wc + 16 * p + 4 * g);
    csu[p] = *(const f32x4*)(cs + F_ + f0 + 48 * wc + 16 * p + 4 * g);
  }
  const char* gp[NI];
#pragma unroll
  for (int i = 0; i < NI; ++i) {
    const int pi = i * 8 + w;
    if (i < 2) {
      const int row = 8 * pi + (lane >> 3);
      const int c = (lane & 7) ^ ((row >> 1) & 7);
      gp[i] = (const char*)(hh + (size_t)(row0 + row) * D_ + c * 8);
    } else {
      const int ci = (pi - 16) * 64 + lane;
      const int pr = ci / 24, pc = ci % 24;
      const int c = pc ^ (BMap<192>::fseg(pr) << 1);
      const u16* src = c < 12 ? wg + f0 + c * 8 : wu + f0 + (c - 12) * 8;
      gp[i] = (const char*)(src + (size_t)pr * F_);
    }
  }
  auto issue = [&](int st_) {
    char LAS* dst = lds + (st_ % NST) * K4_STAGE + w * 1024;
#pragma unroll
    for (int i = 0; i < NI; ++i) glds16(gp[i] + (size_t)st_ * (i < 2 ? 128 : 64 * F_ * 2), dst + i * 8192);
  };
#pragma unroll
  for (int s = 0; s < NST - 1; ++s) issue(s);
  unsigned blo[6], bhi[6];
#pragma unroll
  for (int p = 0; p < 6; ++p) {
    const int cb = (p < 3 ? 0 : 6) + 3 * wc + (p % 3);
    blo[p] = ldsaddr(lds) + 16384 + boff_lo<192>(cb, lane);
    bhi[p] = ldsaddr(lds) + 16384 + boff_hi<192>(cb, lane);
  }
  const int ar = 32 * wr + m;
  const unsigned aa0 = ldsaddr(lds) + ar * 128 + ((g ^ ((ar >> 1) & 7)) * 16);
  const unsigned aa1 = ldsaddr(lds) + ar * 128 + (((4 + g) ^ ((ar >> 1) & 7)) * 16);
  f32x4 acc[2][6];
#pragma unroll
  for (int rb = 0; rb < 2; ++rb)
#pragma unroll
    for (int p = 0; p < 6; ++p) acc[rb][p] = f32x4{0.f, 0.f, 0.f, 0.f};
  bf16x8 a0[2], a1[2];
  s16x4 bl[2][6], bh[2][6];
  sfor<0, NIT>([&](auto tc) {
    constexpr int t = decltype(tc)::value;
    constexpr int issued = (t + NST - 1 < NIT) ? (t + NST - 1) : NIT;
    wait_stage<NI>(issued - t - 1);
    BAR();
    if constexpr (t + NST - 1 < NIT) issue(t + NST - 1);
    const unsigned so = (t % NST) * K4_STAGE;
    auto rd = [&](auto kc) {
      constexpr int kk = decltype(kc)::value;
      a_b128<0>(a0[kk], (kk ? aa1 : aa0) + so);
      a_b128<2048>(a1[kk], (kk ? aa1 : aa0) + so);
#pragma unroll
      for (int p = 0; p < 6; ++p) {
        a_tr<kk * 32 * 384>(bl[kk][p], blo[p] + so);
        a_tr<kk * 32 * 384>(bh[kk][p], bhi[p] + so);
      }
    };
    rd(std::integral_constant<int, 0>{});
    rd(std::integral_constant<int, 1>{});
    sfor<0, 2>([&](auto kc) {
      constexpr int kk = decltype(kc)::value;
      if constexpr (kk == 0) lgkm_wait<14>(); else lgkm_wait<0>();
      pin(a0[kk]); pin(a1[kk]);
#pragma unroll
      for (int p = 0; p < 6; ++p) { pin(bl[kk][p]); pin(bh[kk][p]); }
#pragma unroll
      for (int p = 0; p < 6; ++p) {
        const bf16x8 bfr = cat(bl[kk][p], bh[kk][p]);
        acc[0][p] = MFMA16(bfr, a0[kk], acc[0][p]);
        acc[1][p] = MFMA16(bfr, a1[kk], acc[1][p]);
      }
      __builtin_amdgcn_sched_barrier(0);
    });
  });
#pragma unroll
  for (int rb = 0; rb < 2; ++rb) {
    const float s1 = (st[rb][0][0] + st[rb][0][2]) + (st[rb][1][0] + st[rb][1][2]) + (st[rb][2][0] + st[rb][2][2]) + (st[rb][3][0] + st[rb][3][2]);
    const float s2 = (st[rb][0][1] + st[rb][0][3]) + (st[rb][1][1] + st[rb][1][3]) + (st[rb][2][1] + st[rb][2][3]) + (st[rb][3][1] + st[rb][3][3]);
    const float mean = s1 * (1.0f / 640.0f);
    float var = s2 * (1.0f / 640.0f) - mean * mean;
    var = var < 0.f ? 0.f : var;
    const float rstd = 1.0f / sqrtf(var + LN_EPS);
    u16* dst = gout + (size_t)(row0 + 32 * wr + 16 * rb + m) * F_ + f0 + 48 * wc + 4 * g;
#pragma unroll
    for (int p = 0; p < 3; ++p) {
      float r4[4];
#pragma unroll
      for (int j = 0; j < 4; ++j) {
        const float ga = (acc[rb][p][j] - mean * csg[p][j]) * rstd;
        const float up = (acc[rb][p + 3][j] - mean * csu[p][j]) * rstd;
        r4[j] = gelu_tanh(ga) * up;
      }
      u32x2 o;
      o[0] = pk_bf16(r4[0], r4[1]);
      o[1] = pk_bf16(r4[2], r4[3]);
      *(u32x2*)(dst + 16 * p) = o;
    }
  }
  STAMP(3, 1);
}

extern "C" void kernel_launch(void* const* d_in, const int* in_sizes, int n_in, void* d_out, int out_size,
                              void* d_ws, size_t ws_size, hipStream_t stream) {
  const u16* x = (const u16*)d_in[0];
  const u16* wq = (const u16*)d_in[1];
  const u16* wk = (const u16*)d_in[2];
  const u16* wv = (const u16*)d_in[3];
  const u16* wo = (const u16*)d_in[4];
  const u16* wg = (const u16*)d_in[6];
  const u16* wu = (const u16*)d_in[7];
  const u16* wd = (const u16*)d_in[8];
  char* ws = (char*)d_ws;
  constexpr int K3_LDS = 4 * 36864;
  hipFuncSetAttribute((const void*)k1_qkv, hipFuncAttributeMaxDynamicSharedMemorySize, K1_LDS);
  hipFuncSetAttribute((const void*)k2_attn, hipFuncAttributeMaxDynamicSharedMemorySize, K2_LDS);
  hipFuncSetAttribute((const void*)k3_proj<640, true>, hipFuncAttributeMaxDynamicSharedMemorySize, K3_LDS);
  hipFuncSetAttribute((const void*)k3_proj<1536, false>, hipFuncAttributeMaxDynamicSharedMemorySize, K3_LDS);
  hipFuncSetAttribute((const void*)k4_ffn1, hipFuncAttributeMaxDynamicSharedMemorySize, K4_LDS);
  k1_qkv<<<256, 256, K1_LDS, stream>>>(x, wq, wk, wv, wg, wu, ws);
  k2_attn<<<256, 512, K2_LDS, stream>>>(ws, ws);
  k3_proj<640, true><<<256, 256, K3_LDS, stream>>>((const u16*)(ws + WS_O), wo, x, (u16*)(ws + WS_H),
                                                     (float*)(ws + WS_ST));
  k4_ffn1<<<256, 512, K4_LDS, stream>>>((const u16*)(ws + WS_H), wg, wu, (const float*)(ws + WS_ST),
                                         (const float*)(ws + WS_CS), (u16*)(ws + WS_G));
  k3_proj<1536, false><<<256, 256, K3_LDS, stream>>>((const u16*)(ws + WS_G), wd, (const u16*)(ws + WS_H),
                                                      (u16*)d_out, nullptr);
}
```

```cpp
#include <hip/hip_runtime.h>
#include <hip/hip_bf16.h>
#include <stdint.h>
#include <type_traits>

typedef __attribute__((ext_vector_type(8))) short bf16x8;
typedef __attribute__((ext_vector_type(4))) short s16x4;
typedef __attribute__((ext_vector_type(4))) float f32x4;
typedef __attribute__((ext_vector_type(2))) float f32x2;
typedef __attribute__((ext_vector_type(16))) float f32x16;
typedef __attribute__((ext_vector_type(2))) __bf16 bf16x2_t;
typedef __attribute__((ext_vector_type(4))) unsigned u32x4;
typedef __attribute__((ext_vector_type(2))) unsigned u32x2;
typedef unsigned short u16;
#define LAS __attribute__((address_space(3)))
#define DI __device__ __forceinline__

constexpr int S_ = 2048, D_ = 640, F_ = 1536;
constexpr float QSCALE = 0.22360679774997896f * 1.4426950408889634f;
constexpr float LN_EPS = 1e-5f;
constexpr float FAST_MAX = 40.0f;

constexpr size_t WS_Q = 0;
constexpr size_t WS_KF = WS_Q + 2048 * 640 * 2;
constexpr size_t WS_VF = WS_KF + 64 * 2048;
constexpr size_t WS_KMX = WS_VF + 64 * 2048;
constexpr size_t WS_CS = WS_KMX + 256;
constexpr size_t WS_O = WS_CS + 3072 * 4;
constexpr size_t WS_H = WS_O + 2048 * 640 * 2;
constexpr size_t WS_ST = WS_H + 2048 * 640 * 2;
constexpr size_t WS_G = WS_ST + 2048 * 16 * 4;

#define STAMP(k, i)
DI unsigned pk_bf16(float a, float b) {
  f32x2 v = {a, b};
  return __builtin_bit_cast(unsigned, __builtin_convertvector(v, bf16x2_t));
}
DI float bf_lo(unsigned u) { return __uint_as_float(u << 16); }
DI float bf_hi(unsigned u) { return __uint_as_float(u & 0xffff0000u); }
DI void glds16(const void* g, char LAS* l) {
  __builtin_amdgcn_global_load_lds((const unsigned*)g, (unsigned LAS*)l, 16, 0, 0);
}
DI s16x4 tr_read(const char LAS* p) { return __builtin_amdgcn_ds_read_tr16_b64_v4i16((s16x4 LAS*)p); }
#define MFMA16(a, b, c) __builtin_amdgcn_mfma_f32_16x16x32_bf16((a), (b), (c), 0, 0, 0)
#define MFMA32(a, b, c) __builtin_amdgcn_mfma_f32_32x32x16_bf16((a), (b), (c), 0, 0, 0)
#define WAIT_V(n) asm volatile("s_waitcnt vmcnt(%0)" ::"i"(n) : "memory")
#define BAR() __builtin_amdgcn_s_barrier()

template <int BN> struct BMap;
template <> struct BMap<80> {
  static DI int prow(int k) { return k ^ (((k >> 3) & 1) << 2); }
  static DI int fseg(int k) { return 0; }
};
template <> struct BMap<96> {
  static DI int prow(int k) { return k; }
  static DI int fseg(int k) { return (k >> 3) & 1; }
};
template <> struct BMap<192> {
  static DI int prow(int k) { return k; }
  static DI int fseg(int k) { return ((k >> 1) & 1) | (((k >> 3) & 1) << 1); }
};

template <int BN> DI int boff_lo(int cb, int lane) {
  const int g = lane >> 4, q = (lane >> 2) & 3, p = lane & 3, k = 8 * g + q;
  return BMap<BN>::prow(k) * (2 * BN) + ((cb ^ BMap<BN>::fseg(k)) * 32) + p * 8;
}
template <int BN> DI int boff_hi(int cb, int lane) {
  const int g = lane >> 4, q = (lane >> 2) & 3, p = lane & 3, k = 8 * g + q + 4;
  return BMap<BN>::prow(k) * (2 * BN) + ((cb ^ BMap<BN>::fseg(k)) * 32) + p * 8;
}
DI bf16x8 read_b2(const char LAS* plo, const char LAS* phi) {
  s16x4 lo = tr_read(plo), hi = tr_read(phi);
  return __builtin_shufflevector(lo, hi, 0, 1, 2, 3, 4, 5, 6, 7);
}
DI bf16x8 read_afrag(const char LAS* img, int row, int c) {
  return *(const bf16x8 LAS*)(img + row * 128 + ((c ^ ((row >> 1) & 7)) * 16));
}
template <int I, int N, class F> DI void sfor(F&& f) {
  if constexpr (I < N) {
    f(std::integral_constant<int, I>{});
    sfor<I + 1, N>(f);
  }
}
template <int OFF> DI void a_tr(s16x4& d, unsigned addr) {
  asm volatile("ds_read_b64_tr_b16 %0, %1 offset:%2" : "=v"(d) : "v"(addr), "i"(OFF));
}
template <int OFF> DI void a_b128(bf16x8& d, unsigned addr) {
  asm volatile("ds_read_b128 %0, %1 offset:%2" : "=v"(d) : "v"(addr), "i"(OFF));
}
template <int N> DI void lgkm_wait() { asm volatile("s_waitcnt lgkmcnt(%0)" ::"i"(N) : "memory"); }
DI void pin(bf16x8& a) { asm volatile("" : "+v"(a)); }
DI void pin(s16x4& a) { asm volatile("" : "+v"(a)); }
DI bf16x8 cat(const s16x4& lo, const s16x4& hi) { return __builtin_shufflevector(lo, hi, 0, 1, 2, 3, 4, 5, 6, 7); }
DI unsigned ldsaddr(char LAS* p) { return (unsigned)(size_t)p; }
DI bf16x8 ones8() {
  bf16x8 o;
#pragma unroll
  for (int j = 0; j < 8; ++j) o[j] = 0x3F80;
  return o;
}
template <int N> DI void wait_stage(int pend) {
  if (pend >= 3) WAIT_V(3 * N);
  else if (pend == 2) WAIT_V(2 * N);
  else if (pend == 1) WAIT_V(N);
  else WAIT_V(0);
}

template <int BN, int NST, int NIT, bool STATS>
struct SmallGemm {
  static constexpr int CPR = BN / 8;
  static constexpr int BPIECES = 128 * BN * 2 / 1024;
  static constexpr int NI = (16 + BPIECES) / 4;
  static constexpr int STAGE = (16 + BPIECES) * 1024;
  static constexpr int NCB = BN / 16;

  static DI void run(char LAS* lds, const u16* A, int lda, int row0, const u16* B, int ldb, int n0, int nmax,
                     int rot, f32x4 (&acc)[NCB], f32x4& rs, f32x4& sq, f32x4& cs0, f32x4& cs1, int kid) {
    const int tid = threadIdx.x, lane = tid & 63;
    const int w = __builtin_amdgcn_readfirstlane(tid >> 6);
    const char* gp[NI];
#pragma unroll
    for (int i = 0; i < NI; ++i) {
      const int pi = i * 4 + w;
      if (i < 4) {
        const int sub = pi >> 3, row = 8 * (pi & 7) + (lane >> 3);
        const int c = (lane & 7) ^ ((row >> 1) & 7);
        gp[i] = (const char*)(A + (size_t)(row0 + row) * lda + sub * 64 + c * 8);
      } else {
        const int ci = (pi - 16) * 64 + lane;
        const int pr = ci / CPR, pc = ci % CPR;
        const int k = BMap<BN>::prow(pr);
        const int c = pc ^ (BMap<BN>::fseg(k) << 1);
        int n = n0 + c * 8;
        n = n > nmax - 8 ? nmax - 8 : n;
        gp[i] = (const char*)(B + (size_t)k * ldb + n);
      }
    }
    const int bstep = 128 * ldb * 2;
    auto issue = [&](int st) {
      char LAS* dst = lds + (st % NST) * STAGE + w * 1024;
#pragma unroll
      for (int i = 0; i < NI; ++i)
        glds16(gp[i] + (size_t)st * (i < 4 ? 256 : bstep), dst + i * 4096);
    };
#pragma unroll
    for (int s = 0; s < NST - 1; ++s) issue(s);
    const int arow = 16 * w + (lane & 15), g = lane >> 4;
    unsigned blo[NCB], bhi[NCB];
#pragma unroll
    for (int p = 0; p < NCB; ++p) {
      int cb = p + rot;
      cb = cb >= NCB ? cb - NCB : cb;
      blo[p] = ldsaddr(lds) + 16384 + boff_lo<BN>(cb, lane);
      bhi[p] = ldsaddr(lds) + 16384 + boff_hi<BN>(cb, lane);
    }
    const unsigned aa0 = ldsaddr(lds) + arow * 128 + ((g ^ ((arow >> 1) & 7)) * 16);
    const unsigned aa1 = ldsaddr(lds) + arow * 128 + (((4 + g) ^ ((arow >> 1) & 7)) * 16);
    const bf16x8 ones = ones8();
    bf16x8 af[2];
    s16x4 bl[2][NCB], bh[2][NCB];
    sfor<0, NIT>([&](auto tc) {
      constexpr int t = decltype(tc)::value;
      constexpr int issued = (t + NST - 1 < NIT) ? (t + NST - 1) : NIT;
      wait_stage<NI>(issued - t - 1);
      BAR();
      if constexpr (t == 0) STAMP(kid, 3);
      if constexpr (t == 2) STAMP(kid, 4);
      if constexpr (t + NST - 1 < NIT) issue(t + NST - 1);
      const unsigned so = (t % NST) * STAGE;
      auto rd = [&](auto kc) {
        constexpr int kk = decltype(kc)::value;
        a_b128<(kk >> 1) * 8192>(af[kk & 1], ((kk & 1) ? aa1 : aa0) + so);
#pragma unroll
        for (int p = 0; p < NCB; ++p) {
          a_tr<kk * 32 * 2 * BN>(bl[kk & 1][p], blo[p] + so);
          a_tr<kk * 32 * 2 * BN>(bh[kk & 1][p], bhi[p] + so);
        }
      };
      rd(std::integral_constant<int, 0>{});
      sfor<0, 4>([&](auto kc) {
        constexpr int kk = decltype(kc)::value;
        if constexpr (kk < 3) {
          rd(std::integral_constant<int, kk + 1>{});
          lgkm_wait<1 + 2 * NCB>();
        } else {
          lgkm_wait<0>();
        }
        pin(af[kk & 1]);
#pragma unroll
        for (int p = 0; p < NCB; ++p) { pin(bl[kk & 1][p]); pin(bh[kk & 1][p]); }
        if (STATS) {
          rs = MFMA16(ones, af[kk & 1], rs);
          sq = MFMA16(af[kk & 1], af[kk & 1], sq);
        }
#pragma unroll
        for (int p = 0; p < NCB; ++p) {
          const bf16x8 bfr = cat(bl[kk & 1][p], bh[kk & 1][p]);
          acc[p] = MFMA16(bfr, af[kk & 1], acc[p]);
          if (STATS && p == 0) cs0 = MFMA16(bfr, ones, cs0);
          if (STATS && p == 4) cs1 = MFMA16(bfr, ones, cs1);
        }
        __builtin_amdgcn_sched_barrier(0);
      });
    });
  }
};

DI void finish_stats(const f32x4& rs, const f32x4& sq, int lane, float& mean, float& rstd) {
  const int m = lane & 15;
  const int r = m & 3;
  float d = r == 0 ? sq[0] : (r == 1 ? sq[1] : (r == 2 ? sq[2] : sq[3]));
  d = __shfl(d, (m >> 2) * 16 + m);
  mean = rs[0] * (1.0f / 640.0f);
  float var = d * (1.0f / 640.0f) - mean * mean;
  var = var < 0.f ? 0.f : var;
  rstd = 1.0f / sqrtf(var + LN_EPS);
}

constexpr int K1_LDS = 3 * 40960 + 1024;
__global__ __launch_bounds__(256, 1) void k1_qkv(const u16* __restrict__ x, const u16* __restrict__ wq,
                                                  const u16* __restrict__ wk, const u16* __restrict__ wv,
                                                  char* __restrict__ ws) {
  extern __shared__ __attribute__((aligned(16))) char smem_[];
  char LAS* lds = (char LAS*)smem_;
  const int tid = threadIdx.x, lane = tid & 63;
  STAMP(0, 0);
  const int w = __builtin_amdgcn_readfirstlane(tid >> 6);
  const int bid = blockIdx.x, xcd = bid & 7, idx = bid >> 3;
  const int rt = xcd * 4 + (idx & 3), ct = idx >> 2;
  const int row0 = rt * 64;
  const int g = lane >> 4, m = lane & 15;
  f32x4 z4 = {0.f, 0.f, 0.f, 0.f};
  f32x4 rs = z4, sq = z4;
  float LAS* cs_l = (float LAS*)(lds + 3 * 40960);
  if (ct < 7) {
    using G = SmallGemm<96, 3, 5, true>;
    f32x4 acc[6], cs0 = z4, cs1 = z4;
#pragma unroll
    for (int p = 0; p < 6; ++p) acc[p] = z4;
    G::run(lds, x, D_, row0, wq, D_, ct * 96, D_, w, acc, rs, sq, cs0, cs1, 0);
    STAMP(0, 2);
    float mean, rstd;
    finish_stats(rs, sq, lane, mean, rstd);
    if (m == 0) {
      *(f32x4 LAS*)(cs_l + w * 16 + 4 * g) = cs0;
      if (w < 2) *(f32x4 LAS*)(cs_l + (4 + w) * 16 + 4 * g) = cs1;
    }
    __syncthreads();
    u16* q = (u16*)(ws + WS_Q);
    const float sc = rstd * QSCALE;
#pragma unroll
    for (int p = 0; p < 6; ++p) {
      int cb = p + w;
      cb = cb >= 6 ? cb - 6 : cb;
      const int n = ct * 96 + cb * 16 + 4 * g;
      const f32x4 c = *(const f32x4 LAS*)(cs_l + cb * 16 + 4 * g);
      if (n < D_) {
        u32x2 o;
        o[0] = pk_bf16((acc[p][0] - mean * c[0]) * sc, (acc[p][1] - mean * c[1]) * sc);
        o[1] = pk_bf16((acc[p][2] - mean * c[2]) * sc, (acc[p][3] - mean * c[3]) * sc);
        *(u32x2*)(q + (size_t)(row0 + 16 * w + m) * D_ + n) = o;
      }
    }
  } else {
    constexpr int NI = 7, STAGE = 28 * 1024, NST = 4, NIT = 5;
    const char* gp[NI];
#pragma unroll
    for (int i = 0; i < NI; ++i) {
      const int pi = i * 4 + w;
      if (i < 4) {
        const int sub = pi >> 3, row = 8 * (pi & 7) + (lane >> 3);
        const int c = (lane & 7) ^ ((row >> 1) & 7);
        gp[i] = (const char*)(x + (size_t)(row0 + row) * D_ + sub * 64 + c * 8);
      } else {
        int pj = pi - 16;
        pj = pj >= 10 ? pj - 10 : pj;
        const u16* src = pj < 5 ? wk : wv;
        gp[i] = (const char*)src + (pj < 5 ? pj : pj - 5) * 1024 + lane * 16;
      }
    }
    auto issue = [&](int st) {
      char LAS* dst = lds + (st % NST) * STAGE + w * 1024;
#pragma unroll
      for (int i = 0; i < NI; ++i) glds16(gp[i] + (size_t)st * (i < 4 ? 256 : 5120), dst + i * 4096);
    };
#pragma unroll
    for (int s = 0; s < NST - 1; ++s) issue(s);
    const int arow = 16 * w + m;
    const int q4 = (lane >> 2) & 3, p4 = lane & 3;
    const int bo = (8 * g + q4) * 40 + p4 * 8;
    f32x4 ak0 = z4, ak1 = z4, av0 = z4, av1 = z4, ck0 = z4, ck1 = z4, cv0 = z4, cv1 = z4;
    const bf16x8 ones = ones8();
    const unsigned aa0 = ldsaddr(lds) + arow * 128 + ((g ^ ((arow >> 1) & 7)) * 16);
    const unsigned aa1 = ldsaddr(lds) + arow * 128 + (((4 + g) ^ ((arow >> 1) & 7)) * 16);
    const unsigned bb = ldsaddr(lds) + 16384 + bo;
    sfor<0, NIT>([&](auto tc) {
      constexpr int t = decltype(tc)::value;
      constexpr int issued = (t + NST - 1 < NIT) ? (t + NST - 1) : NIT;
      wait_stage<NI>(issued - t - 1);
      BAR();
      if constexpr (t + NST - 1 < NIT) issue(t + NST - 1);
      const unsigned so = (t % NST) * STAGE;
      sfor<0, 4>([&](auto kc) {
        constexpr int kk = decltype(kc)::value;
        bf16x8 af;
        s16x4 r[8];
        a_b128<(kk >> 1) * 8192>(af, ((kk & 1) ? aa1 : aa0) + so);
        constexpr int o = kk * 32 * 40;
        a_tr<o>(r[0], bb + so); a_tr<o + 160>(r[1], bb + so); a_tr<o + 32>(r[2], bb + so); a_tr<o + 192>(r[3], bb + so);
        a_tr<5120 + o>(r[4], bb + so); a_tr<5120 + o + 160>(r[5], bb + so);
        a_tr<5120 + o + 32>(r[6], bb + so); a_tr<5120 + o + 192>(r[7], bb + so);
        lgkm_wait<0>();
        pin(af);
#pragma unroll
        for (int j = 0; j < 8; ++j) pin(r[j]);
        const bf16x8 k0 = cat(r[0], r[1]), k1 = cat(r[2], r[3]), v0 = cat(r[4], r[5]), v1 = cat(r[6], r[7]);
        rs = MFMA16(ones, af, rs);
        sq = MFMA16(af, af, sq);
        ak0 = MFMA16(k0, af, ak0);
        ak1 = MFMA16(k1, af, ak1);
        ck0 = MFMA16(k0, ones, ck0);
        ck1 = MFMA16(k1, ones, ck1);
        av0 = MFMA16(af, v0, av0);
        av1 = MFMA16(af, v1, av1);
        cv0 = MFMA16(ones, v0, cv0);
        cv1 = MFMA16(ones, v1, cv1);
      });
    });
    float mean, rstd;
    finish_stats(rs, sq, lane, mean, rstd);
    u16* kf = (u16*)(ws + WS_KF);
    u16* vf = (u16*)(ws + WS_VF);
    const int key = row0 + 16 * w + m;
    const int blk = key >> 5, kk5 = key & 31;
    const int r = (kk5 & 19) | ((kk5 & 4) << 1) | ((kk5 & 8) >> 1);
    float kv[4], n2;
    u32x2 o;
#pragma unroll
    for (int j = 0; j < 4; ++j) kv[j] = (ak0[j] - mean * ck0[j]) * rstd;
    o[0] = pk_bf16(kv[0], kv[1]);
    o[1] = pk_bf16(kv[2], kv[3]);
    n2 = bf_lo(o[0]) * bf_lo(o[0]) + bf_hi(o[0]) * bf_hi(o[0]) + bf_lo(o[1]) * bf_lo(o[1]) + bf_hi(o[1]) * bf_hi(o[1]);
    *(u32x2*)(kf + (size_t)blk * 1024 + ((g >> 1) * 32 + r) * 8 + (g & 1) * 4) = o;
    {
      u32x2 o1;
#pragma unroll
      for (int j = 0; j < 4; ++j) kv[j] = (ak1[j] - mean * ck1[j]) * rstd;
      o1[0] = pk_bf16(kv[0], kv[1]);
      o1[1] = pk_bf16(kv[2], kv[3]);
      if (g == 0)
        n2 += bf_lo(o1[0]) * bf_lo(o1[0]) + bf_hi(o1[0]) * bf_hi(o1[0]) + bf_lo(o1[1]) * bf_lo(o1[1]) +
              bf_hi(o1[1]) * bf_hi(o1[1]);
      if (g == 1) { o1[0] = 0x3F803F80u; o1[1] = 0x00003F80u; }
      if (g >= 2) { o1[0] = 0u; o1[1] = 0u; }
      *(u32x2*)(kf + (size_t)blk * 1024 + 512 + ((g >> 1) * 32 + r) * 8 + (g & 1) * 4) = o1;
    }
    n2 += __shfl_xor(n2, 16);
    n2 += __shfl_xor(n2, 32);
#pragma unroll
    for (int s = 1; s < 16; s <<= 1) n2 = fmaxf(n2, __shfl_xor(n2, s));
    float LAS* red = (float LAS*)(lds + 4 * 28 * 1024);
    if (lane == 0) red[w] = n2;
    {
      float mj[4], rj[4];
#pragma unroll
      for (int j = 0; j < 4; ++j) {
        mj[j] = __shfl(mean, 4 * g + j);
        rj[j] = __shfl(rstd, 4 * g + j);
      }
      const int key0 = row0 + 16 * w + 4 * g;
      const int vb = key0 >> 5, k5 = key0 & 31;
      const int s = k5 >> 4, h = (k5 >> 3) & 1, j0 = k5 & 7;
      u16* base = vf + (size_t)vb * 1024 + s * 512 + h * 256 + j0;
      u32x2 ov;
      ov[0] = pk_bf16((av0[0] - mj[0] * cv0[0]) * rj[0], (av0[1] - mj[1] * cv0[1]) * rj[1]);
      ov[1] = pk_bf16((av0[2] - mj[2] * cv0[2]) * rj[2], (av0[3] - mj[3] * cv0[3]) * rj[3]);
      *(u32x2*)(base + m * 8) = ov;
      ov[0] = pk_bf16((av1[0] - mj[0] * cv1[0]) * rj[0], (av1[1] - mj[1] * cv1[1]) * rj[1]);
      ov[1] = pk_bf16((av1[2] - mj[2] * cv1[2]) * rj[2], (av1[3] - mj[3] * cv1[3]) * rj[3]);
      if (m == 4) { ov[0] = 0x3F803F80u; ov[1] = 0x3F803F80u; }
      if (m > 4) { ov[0] = 0u; ov[1] = 0u; }
      *(u32x2*)(base + (16 + m) * 8) = ov;
    }
    __syncthreads();
    if (tid == 0) ((float*)(ws + WS_KMX))[rt] = fmaxf(fmaxf(red[0], red[1]), fmaxf(red[2], red[3]));
  }
  STAMP(0, 1);
}

constexpr int K2_LDS = 4 * 256 * 96 + 4096;
DI void split3(float M, unsigned& s01, unsigned& s2) {
  const float n = -M;
  const unsigned hi = __float_as_uint(n) & 0xffff0000u;
  const float r1 = n - __uint_as_float(hi);
  const unsigned mid = __float_as_uint(r1) & 0xffff0000u;
  const float r2 = r1 - __uint_as_float(mid);
  const unsigned lo = __float_as_uint(r2) & 0xffff0000u;
  s01 = (hi >> 16) | mid;
  s2 = lo >> 16;
}
__global__ __launch_bounds__(512, 2) void k2_attn(const char* __restrict__ wsr, char* __restrict__ ws,
                                                   const char* __restrict__ wo, const char* __restrict__ wg,
                                                   const char* __restrict__ wu, const char* __restrict__ wd) {
  extern __shared__ __attribute__((aligned(16))) char smem_[];
  char LAS* lds = (char LAS*)smem_;
  const int tid = threadIdx.x, lane = tid & 63;
  STAMP(1, 0);
  const int w = __builtin_amdgcn_readfirstlane(tid >> 6);
  const int ch = w & 1, ks = w >> 1;
  const int r = lane & 31, h = lane >> 5;
  const int qrow0 = blockIdx.x * 8;
  const u16* q = (const u16*)(wsr + WS_Q);
  const bf16x8* kf = (const bf16x8*)(wsr + WS_KF) + (size_t)ks * 16 * 128 + lane;
  const bf16x8* vf = (const bf16x8*)(wsr + WS_VF) + (size_t)ks * 16 * 128 + lane;
  bf16x8 q0[4], q1[4];
  float qn[4];
#pragma unroll
  for (int gq = 0; gq < 4; ++gq) {
    const u16* src = q + (size_t)(qrow0 + ch * 4 + gq) * D_ + r * 20;
    const u32x2 a = *(const u32x2*)(src + 8 * h), b = *(const u32x2*)(src + 8 * h + 4);
    u32x2 c = *(const u32x2*)(src + 16);
    if (h) { c[0] = 0u; c[1] = 0u; }
    u32x4 f0 = {a[0], a[1], b[0], b[1]};
    u32x4 f1 = {c[0], c[1], 0u, 0u};
    float n2 = 0.f;
#pragma unroll
    for (int e = 0; e < 4; ++e) n2 += bf_lo(f0[e]) * bf_lo(f0[e]) + bf_hi(f0[e]) * bf_hi(f0[e]);
#pragma unroll
    for (int e = 0; e < 2; ++e) n2 += bf_lo(f1[e]) * bf_lo(f1[e]) + bf_hi(f1[e]) * bf_hi(f1[e]);
    n2 += __shfl_xor(n2, 32);
    qn[gq] = n2;
    q0[gq] = __builtin_bit_cast(bf16x8, f0);
    q1[gq] = __builtin_bit_cast(bf16x8, f1);
  }
  float kmx = ((const float*)(wsr + WS_KMX))[r];
#pragma unroll
  for (int s = 1; s < 32; s <<= 1) kmx = fmaxf(kmx, __shfl_xor(kmx, s));
  float M[4];
  bool unsafe = false;
#pragma unroll
  for (int gq = 0; gq < 4; ++gq) {
    M[gq] = sqrtf(qn[gq] * kmx) * 1.001f + 1e-3f;
    unsafe |= !(M[gq] <= FAST_MAX);
  }
  f32x16 z16;
#pragma unroll
  for (int i = 0; i < 16; ++i) z16[i] = 0.f;
  if (__syncthreads_or(unsafe)) {
    float mx[4] = {-INFINITY, -INFINITY, -INFINITY, -INFINITY};
    for (int b = 0; b < 16; ++b) {
      const bf16x8 k0 = kf[b * 128], k1 = kf[b * 128 + 64];
#pragma unroll
      for (int gq = 0; gq < 4; ++gq) {
        f32x16 s = MFMA32(k0, q0[gq], z16);
        s = MFMA32(k1, q1[gq], s);
#pragma unroll
        for (int i = 0; i < 16; ++i) mx[gq] = fmaxf(mx[gq], s[i]);
      }
    }
    float LAS* mred = (float LAS*)lds;
#pragma unroll
    for (int gq = 0; gq < 4; ++gq) {
      mx[gq] = fmaxf(mx[gq], __shfl_xor(mx[gq], 32));
      if (h == 0) mred[ks * 256 + ch * 128 + gq * 32 + r] = mx[gq];
    }
    __syncthreads();
#pragma unroll
    for (int gq = 0; gq < 4; ++gq) {
      const int c = ch * 128 + gq * 32 + r;
      M[gq] = fmaxf(fmaxf(mred[c], mred[256 + c]), fmaxf(mred[512 + c], mred[768 + c]));
    }
    __syncthreads();
  }
#pragma unroll
  for (int gq = 0; gq < 4; ++gq) {
    unsigned s01, s2;
    split3(M[gq], s01, s2);
    u32x4 f1 = __builtin_bit_cast(u32x4, q1[gq]);
    if (h == 0) { f1[2] = s01; f1[3] = s2; }
    q1[gq] = __builtin_bit_cast(bf16x8, f1);
  }
  STAMP(1, 2);
  f32x16 o[4];
#pragma unroll
  for (int gq = 0; gq < 4; ++gq) o[gq] = z16;
  {
    bf16x8 kA0, kA1, vA0, vA1, kB0, kB1, vB0, vB1;
    f32x16 sA, sB;
    u32x4 p0, p1 = {0u, 0u, 0u, 0u};
    auto ld2 = [&](bf16x8& a0, bf16x8& a1, const bf16x8* p) {
      asm volatile("global_load_dwordx4 %0, %1, off" : "=v"(a0) : "v"(p));
      asm volatile("global_load_dwordx4 %0, %1, off offset:1024" : "=v"(a1) : "v"(p));
    };
#define SB() __builtin_amdgcn_sched_barrier(0)
#define VW2(n, a, b) asm volatile("s_waitcnt vmcnt(" #n ")" : "+v"(a), "+v"(b))
#define EX2(dst, j, sc, i)                                                                               \
    dst[j] = pk_bf16(__builtin_amdgcn_exp2f(sc[i]), __builtin_amdgcn_exp2f(sc[i + 1]));                  \
    dst[j + 1] = pk_bf16(__builtin_amdgcn_exp2f(sc[i + 2]), __builtin_amdgcn_exp2f(sc[i + 3]));
#define SLOT(sc, sn, nk0, nk1, gn, gp, pvv1, gq, cv0, PRE1, POST2, POST3, PRE4) \
    PRE1                                                                        \
    sn = MFMA32(nk0, q0[gn], z16); SB();                                        \
    EX2(p0, 0, sc, 0) SB();                                                     \
    o[gp] = MFMA32(pvv1, __builtin_bit_cast(bf16x8, p1), o[gp]); SB();          \
    POST2                                                                       \
    EX2(p0, 2, sc, 4) SB();                                                     \
    sn = MFMA32(nk1, q1[gn], sn); SB();                                         \
    POST3                                                                       \
    EX2(p1, 0, sc, 8) SB();                                                     \
    PRE4                                                                        \
    o[gq] = MFMA32(cv0, __builtin_bit_cast(bf16x8, p0), o[gq]); SB();           \
    EX2(p1, 2, sc, 12) SB();
#define BLOCK(ck0, ck1, cv0, cv1, nk0, nk1, nv0, nv1, b1, b2)                                             \
    SLOT(sA, sB, ck0, ck1, 1, 3, nv1, 0, cv0, , ld2(nv0, nv1, vf + (b1) * 128);, , VW2(4, cv0, cv1);)     \
    SLOT(sB, sA, ck0, ck1, 2, 0, cv1, 1, cv0, , , , )                                                     \
    SLOT(sA, sB, ck0, ck1, 3, 1, cv1, 2, cv0, , , ld2(ck0, ck1, kf + (b2) * 128);, )                      \
    SLOT(sB, sA, nk0, nk1, 0, 2, cv1, 3, cv0, VW2(4, nk0, nk1);, , , )
    ld2(kA0, kA1, kf);
    ld2(vA0, vA1, vf);
    ld2(kB0, kB1, kf + 128);
#pragma unroll
    for (int j = 0; j < 8; ++j) vB1[j] = 0;
    VW2(4, kA0, kA1);
    sA = MFMA32(kA0, q0[0], z16);
    sA = MFMA32(kA1, q1[0], sA);
    for (int b = 0; b < 16; b += 2) {
      BLOCK(kA0, kA1, vA0, vA1, kB0, kB1, vB0, vB1, b + 1, (b + 2 < 16 ? b + 2 : 15))
      BLOCK(kB0, kB1, vB0, vB1, kA0, kA1, vA0, vA1, (b + 2 < 16 ? b + 2 : 15), (b + 3 < 16 ? b + 3 : 15))
    }
    o[3] = MFMA32(vB1, __builtin_bit_cast(bf16x8, p1), o[3]);
    asm volatile("s_waitcnt vmcnt(0)" ::: "memory");
  }
  STAMP(1, 3);
#define PF_MODE 1
  if (w < 4) {
    constexpr int NSLOT = PF_MODE == 1 ? 1024 : 128, NPC = 3 * 1920 + 800, NIT = (NPC + NSLOT - 1) / NSLOT;
    const int slot = (PF_MODE == 1 ? blockIdx.x : (blockIdx.x >> 3)) * 4 + w;
    unsigned acc_ = 0;
#pragma unroll 8
    for (int i = 0; i < NIT; ++i) {
      int pi = slot + i * NSLOT;
      pi = pi < NPC ? pi : slot;
      const char* base = pi < 1920 ? wd : (pi < 3840 ? wg : (pi < 5760 ? wu : wo));
      const int off = pi < 1920 ? pi : (pi < 3840 ? pi - 1920 : (pi < 5760 ? pi - 3840 : pi - 5760));
      const u32x4 v = *(const u32x4*)(base + (size_t)off * 1024 + lane * 16);
      acc_ ^= v[0];
    }
    asm volatile("" ::"v"(acc_));
  }
  float LAS* red = (float LAS*)lds;
#pragma unroll
  for (int gq = 0; gq < 4; ++gq) {
    float LAS* dst = red + ((size_t)ks * 256 + ch * 128 + gq * 32 + r) * 24 + 4 * h;
#pragma unroll
    for (int t = 0; t < 3; ++t) {
      f32x4 v = {o[gq][4 * t], o[gq][4 * t + 1], o[gq][4 * t + 2], o[gq][4 * t + 3]};
      *(f32x4 LAS*)(dst + 8 * t) = v;
    }
  }
  __syncthreads();
  u16* og = (u16*)(ws + WS_O) + (size_t)qrow0 * D_;
  for (int it = tid; it < 1280; it += 512) {
    const int c = it / 5, qd = it % 5;
    f32x4 a = {0.f, 0.f, 0.f, 0.f};
    float l = 0.f;
#pragma unroll
    for (int s = 0; s < 4; ++s) {
      const float LAS* src = red + ((size_t)s * 256 + c) * 24;
      a += *(const f32x4 LAS*)(src + 4 * qd);
      l += src[20];
    }
    const float inv = 1.0f / l;
    u32x2 ov;
    ov[0] = pk_bf16(a[0] * inv, a[1] * inv);
    ov[1] = pk_bf16(a[2] * inv, a[3] * inv);
    *(u32x2*)(og + (size_t)it * 4) = ov;
  }
  STAMP(1, 1);
}

template <int KDIM, bool STATS_OUT>
__global__ __launch_bounds__(256, 1) void k3_proj(const u16* __restrict__ A, const u16* __restrict__ W,
                                                   const u16* __restrict__ res, u16* __restrict__ out,
                                                   float* __restrict__ stats) {
  extern __shared__ __attribute__((aligned(16))) char smem_[];
  char LAS* lds = (char LAS*)smem_;
  const int tid = threadIdx.x, lane = tid & 63;
  STAMP(STATS_OUT ? 2 : 4, 0);
  const int w = __builtin_amdgcn_readfirstlane(tid >> 6);
  const int bid = blockIdx.x, xcd = bid & 7, idx = bid >> 3;
  const int rt = xcd * 4 + (idx & 3), ct = idx >> 2;
  const int g = lane >> 4, m = lane & 15;
  const int row = rt * 64 + 16 * w + m;
  u32x2 rv[5];
#pragma unroll
  for (int p = 0; p < 5; ++p) rv[p] = *(const u32x2*)(res + (size_t)row * D_ + ct * 80 + p * 16 + 4 * g);
  using G = SmallGemm<80, 4, KDIM / 128, false>;
  f32x4 z4 = {0.f, 0.f, 0.f, 0.f};
  f32x4 acc[5], d0 = z4, d1 = z4, d2 = z4, d3 = z4;
#pragma unroll
  for (int p = 0; p < 5; ++p) acc[p] = z4;
  G::run(lds, A, KDIM, rt * 64, W, D_, ct * 80, D_, 0, acc, d0, d1, d2, d3, STATS_OUT ? 2 : 4);
  STAMP(STATS_OUT ? 2 : 4, 2);
  float s1 = 0.f, s2 = 0.f;
#pragma unroll
  for (int p = 0; p < 5; ++p) {
    u32x2 o;
    o[0] = pk_bf16(bf_lo(rv[p][0]) + acc[p][0], bf_hi(rv[p][0]) + acc[p][1]);
    o[1] = pk_bf16(bf_lo(rv[p][1]) + acc[p][2], bf_hi(rv[p][1]) + acc[p][3]);
    *(u32x2*)(out + (size_t)row * D_ + ct * 80 + p * 16 + 4 * g) = o;
    if (STATS_OUT) {
      const float a = bf_lo(o[0]), b = bf_hi(o[0]), c = bf_lo(o[1]), d = bf_hi(o[1]);
      s1 += (a + b) + (c + d);
      s2 += (a * a + b * b) + (c * c + d * d);
    }
  }
  if (STATS_OUT) {
    s1 += __shfl_xor(s1, 16); s1 += __shfl_xor(s1, 32);
    s2 += __shfl_xor(s2, 16); s2 += __shfl_xor(s2, 32);
    if (g == 0) { f32x2 v = {s1, s2}; *(f32x2*)(stats + ((size_t)row * 8 + ct) * 2) = v; }
  }
  STAMP(STATS_OUT ? 2 : 4, 1);
}

constexpr int K4_STAGE = 40960, K4_LDS = 3 * K4_STAGE;
DI float gelu_tanh(float x) {
  const float u = 0.7978845608028654f * (x + 0.044715f * x * x * x);
  const float e = __builtin_amdgcn_exp2f(u * 2.8853900817779268f);
  const float t = 1.0f - 2.0f * __builtin_amdgcn_rcpf(e + 1.0f);
  return 0.5f * x * (1.0f + t);
}
__global__ __launch_bounds__(512, 2) void k4_ffn1(const u16* __restrict__ hh, const u16* __restrict__ wg,
                                                   const u16* __restrict__ wu, const float* __restrict__ stats,
                                                   u16* __restrict__ gout) {
  extern __shared__ __attribute__((aligned(16))) char smem_[];
  char LAS* lds = (char LAS*)smem_;
  const int tid = threadIdx.x, lane = tid & 63;
  STAMP(3, 0);
  const int w = __builtin_amdgcn_readfirstlane(tid >> 6);
  const int bid = blockIdx.x, xcd = bid & 7, idx = bid >> 3;
  const int rt = (xcd >> 1) * 4 + (idx & 3), ct = (xcd & 1) * 8 + (idx >> 2);
  const int row0 = rt * 128, f0 = ct * 96;
  const int wr = w >> 1, wc = w & 1;
  const int g = lane >> 4, m = lane & 15;
  constexpr int NI = 5, NST = 3, NIT = 10;
  f32x4 st[2][4];
#pragma unroll
  for (int rb = 0; rb < 2; ++rb)
#pragma unroll
    for (int j = 0; j < 4; ++j)
      st[rb][j] = *(const f32x4*)(stats + ((size_t)(row0 + 32 * wr + 16 * rb + m) * 8 + 2 * j) * 2);
  const int rot = wr >= 3 ? 0 : wr;
  const char* gp[NI];
#pragma unroll
  for (int i = 0; i < NI; ++i) {
    const int pi = i * 8 + w;
    if (i < 2) {
      const int row = 8 * pi + (lane >> 3);
      const int c = (lane & 7) ^ ((row >> 1) & 7);
      gp[i] = (const char*)(hh + (size_t)(row0 + row) * D_ + c * 8);
    } else {
      const int ci = (pi - 16) * 64 + lane;
      const int pr = ci / 24, pc = ci % 24;
      const int c = pc ^ (BMap<192>::fseg(pr) << 1);
      const u16* src = c < 12 ? wg + f0 + c * 8 : wu + f0 + (c - 12) * 8;
      gp[i] = (const char*)(src + (size_t)pr * F_);
    }
  }
  auto issue = [&](int st_) {
    char LAS* dst = lds + (st_ % NST) * K4_STAGE + w * 1024;
#pragma unroll
    for (int i = 0; i < NI; ++i) glds16(gp[i] + (size_t)st_ * (i < 2 ? 128 : 64 * F_ * 2), dst + i * 8192);
  };
#pragma unroll
  for (int s = 0; s < NST - 1; ++s) issue(s);
  unsigned blo[6], bhi[6];
#pragma unroll
  for (int p = 0; p < 6; ++p) {
    int c3 = (p % 3) + rot;
    c3 = c3 >= 3 ? c3 - 3 : c3;
    const int cb = (p < 3 ? 0 : 6) + 3 * wc + c3;
    blo[p] = ldsaddr(lds) + 16384 + boff_lo<192>(cb, lane);
    bhi[p] = ldsaddr(lds) + 16384 + boff_hi<192>(cb, lane);
  }
  const int ar = 32 * wr + m;
  const unsigned aa0 = ldsaddr(lds) + ar * 128 + ((g ^ ((ar >> 1) & 7)) * 16);
  const unsigned aa1 = ldsaddr(lds) + ar * 128 + (((4 + g) ^ ((ar >> 1) & 7)) * 16);
  f32x4 acc[2][6];
#pragma unroll
  for (int rb = 0; rb < 2; ++rb)
#pragma unroll
    for (int p = 0; p < 6; ++p) acc[rb][p] = f32x4{0.f, 0.f, 0.f, 0.f};
  bf16x8 a0[2], a1[2];
  s16x4 bl[2][6], bh[2][6];
  f32x4 cg = {0.f, 0.f, 0.f, 0.f}, cu = {0.f, 0.f, 0.f, 0.f};
  const bf16x8 ones = ones8();
  sfor<0, NIT>([&](auto tc) {
    constexpr int t = decltype(tc)::value;
    constexpr int issued = (t + NST - 1 < NIT) ? (t + NST - 1) : NIT;
    wait_stage<NI>(issued - t - 1);
    BAR();
    if constexpr (t == 0) STAMP(3, 3);
    if constexpr (t == 2) STAMP(3, 4);
    if constexpr (t + NST - 1 < NIT) issue(t + NST - 1);
    const unsigned so = (t % NST) * K4_STAGE;
    auto rd = [&](auto kc) {
      constexpr int kk = decltype(kc)::value;
      a_b128<0>(a0[kk], (kk ? aa1 : aa0) + so);
      a_b128<2048>(a1[kk], (kk ? aa1 : aa0) + so);
#pragma unroll
      for (int p = 0; p < 6; ++p) {
        a_tr<kk * 32 * 384>(bl[kk][p], blo[p] + so);
        a_tr<kk * 32 * 384>(bh[kk][p], bhi[p] + so);
      }
    };
    rd(std::integral_constant<int, 0>{});
    rd(std::integral_constant<int, 1>{});
    sfor<0, 2>([&](auto kc) {
      constexpr int kk = decltype(kc)::value;
      if constexpr (kk == 0) lgkm_wait<14>(); else lgkm_wait<0>();
      pin(a0[kk]); pin(a1[kk]);
#pragma unroll
      for (int p = 0; p < 6; ++p) { pin(bl[kk][p]); pin(bh[kk][p]); }
#pragma unroll
      for (int p = 0; p < 6; ++p) {
        const bf16x8 bfr = cat(bl[kk][p], bh[kk][p]);
        acc[0][p] = MFMA16(bfr, a0[kk], acc[0][p]);
        acc[1][p] = MFMA16(bfr, a1[kk], acc[1][p]);
        if (p == 0) cg = MFMA16(bfr, ones, cg);
        if (p == 3) cu = MFMA16(bfr, ones, cu);
      }
      __builtin_amdgcn_sched_barrier(0);
    });
  });
  STAMP(3, 2);
  float LAS* csl = (float LAS*)(lds + K4_STAGE);
  if (m == 0 && wr < 3) {
    *(f32x4 LAS*)(csl + wc * 96 + rot * 16 + 4 * g) = cg;
    *(f32x4 LAS*)(csl + wc * 96 + 48 + rot * 16 + 4 * g) = cu;
  }
  __syncthreads();
  f32x4 csg[3], csu[3];
#pragma unroll
  for (int p = 0; p < 3; ++p) {
    int c3 = p + rot;
    c3 = c3 >= 3 ? c3 - 3 : c3;
    csg[p] = *(const f32x4 LAS*)(csl + wc * 96 + c3 * 16 + 4 * g);
    csu[p] = *(const f32x4 LAS*)(csl + wc * 96 + 48 + c3 * 16 + 4 * g);
  }
#pragma unroll
  for (int rb = 0; rb < 2; ++rb) {
    const float s1 = (st[rb][0][0] + st[rb][0][2]) + (st[rb][1][0] + st[rb][1][2]) + (st[rb][2][0] + st[rb][2][2]) + (st[rb][3][0] + st[rb][3][2]);
    const float s2 = (st[rb][0][1] + st[rb][0][3]) + (st[rb][1][1] + st[rb][1][3]) + (st[rb][2][1] + st[rb][2][3]) + (st[rb][3][1] + st[rb][3][3]);
    const float mean = s1 * (1.0f / 640.0f);
    float var = s2 * (1.0f / 640.0f) - mean * mean;
    var = var < 0.f ? 0.f : var;
    const float rstd = 1.0f / sqrtf(var + LN_EPS);
    u16* dst = gout + (size_t)(row0 + 32 * wr + 16 * rb + m) * F_ + f0 + 48 * wc + 4 * g;
#pragma unroll
    for (int p = 0; p < 3; ++p) {
      float r4[4];
#pragma unroll
      for (int j = 0; j < 4; ++j) {
        const float ga = (acc[rb][p][j] - mean * csg[p][j]) * rstd;
        const float up = (acc[rb][p + 3][j] - mean * csu[p][j]) * rstd;
        r4[j] = gelu_tanh(ga) * up;
      }
      u32x2 o;
      o[0] = pk_bf16(r4[0], r4[1]);
      o[1] = pk_bf16(r4[2], r4[3]);
      int c3 = p + rot;
      c3 = c3 >= 3 ? c3 - 3 : c3;
      *(u32x2*)(dst + 16 * c3) = o;
    }
  }
  STAMP(3, 1);
}

extern "C" void kernel_launch(void* const* d_in, const int* in_sizes, int n_in, void* d_out, int out_size,
                              void* d_ws, size_t ws_size, hipStream_t stream) {
  const u16* x = (const u16*)d_in[0];
  const u16* wq = (const u16*)d_in[1];
  const u16* wk = (const u16*)d_in[2];
  const u16* wv = (const u16*)d_in[3];
  const u16* wo = (const u16*)d_in[4];
  const u16* wg = (const u16*)d_in[6];
  const u16* wu = (const u16*)d_in[7];
  const u16* wd = (const u16*)d_in[8];
  char* ws = (char*)d_ws;
  constexpr int K3_LDS = 4 * 36864;
  hipFuncSetAttribute((const void*)k1_qkv, hipFuncAttributeMaxDynamicSharedMemorySize, K1_LDS);
  hipFuncSetAttribute((const void*)k2_attn, hipFuncAttributeMaxDynamicSharedMemorySize, K2_LDS);
  hipFuncSetAttribute((const void*)k3_proj<640, true>, hipFuncAttributeMaxDynamicSharedMemorySize, K3_LDS);
  hipFuncSetAttribute((const void*)k3_proj<1536, false>, hipFuncAttributeMaxDynamicSharedMemorySize, K3_LDS);
  hipFuncSetAttribute((const void*)k4_ffn1, hipFuncAttributeMaxDynamicSharedMemorySize, K4_LDS);
  k1_qkv<<<256, 256, K1_LDS, stream>>>(x, wq, wk, wv, ws);
  k2_attn<<<256, 512, K2_LDS, stream>>>(ws, ws, (const char*)wo, (const char*)wg, (const char*)wu, (const char*)wd);
  k3_proj<640, true><<<256, 256, K3_LDS, stream>>>((const u16*)(ws + WS_O), wo, x, (u16*)(ws + WS_H),
                                                     (float*)(ws + WS_ST));
  k4_ffn1<<<256, 512, K4_LDS, stream>>>((const u16*)(ws + WS_H), wg, wu, (const float*)(ws + WS_ST),
                                         (u16*)(ws + WS_G));
  k3_proj<1536, false><<<256, 256, K3_LDS, stream>>>((const u16*)(ws + WS_G), wd, (const u16*)(ws + WS_H),
                                                      (u16*)d_out, nullptr);
}
```

```cpp
#include <hip/hip_runtime.h>
#include <hip/hip_bf16.h>
#include <stdint.h>
#include <type_traits>

typedef __attribute__((ext_vector_type(8))) short bf16x8;
typedef __attribute__((ext_vector_type(4))) short s16x4;
typedef __attribute__((ext_vector_type(4))) float f32x4;
typedef __attribute__((ext_vector_type(2))) float f32x2;
typedef __attribute__((ext_vector_type(16))) float f32x16;
typedef __attribute__((ext_vector_type(2))) __bf16 bf16x2_t;
typedef __attribute__((ext_vector_type(4))) unsigned u32x4;
typedef __attribute__((ext_vector_type(2))) unsigned u32x2;
typedef unsigned short u16;
#define LAS __attribute__((address_space(3)))
#define DI __device__ __forceinline__

constexpr int S_ = 2048, D_ = 640, F_ = 1536;
constexpr float QSCALE = 0.22360679774997896f * 1.4426950408889634f;
constexpr float LN_EPS = 1e-5f;
constexpr float FAST_MAX = 40.0f;

constexpr size_t WS_Q = 0;
constexpr size_t WS_KF = WS_Q + 2048 * 640 * 2;
constexpr size_t WS_VF = WS_KF + 64 * 2048;
constexpr size_t WS_KMX = WS_VF + 64 * 2048;
constexpr size_t WS_CS = WS_KMX + 256;
constexpr size_t WS_O = WS_CS + 3072 * 4;
constexpr size_t WS_H = WS_O + 2048 * 640 * 2;
constexpr size_t WS_ST = WS_H + 2048 * 640 * 2;
constexpr size_t WS_G = WS_ST + 2048 * 16 * 4;

#define STAMP(k, i)
DI unsigned pk_bf16(float a, float b) {
  f32x2 v = {a, b};
  return __builtin_bit_cast(unsigned, __builtin_convertvector(v, bf16x2_t));
}
DI float bf_lo(unsigned u) { return __uint_as_float(u << 16); }
DI float bf_hi(unsigned u) { return __uint_as_float(u & 0xffff0000u); }
DI void glds16(const void* g, char LAS* l) {
  __builtin_amdgcn_global_load_lds((const unsigned*)g, (unsigned LAS*)l, 16, 0, 0);
}
DI s16x4 tr_read(const char LAS* p) { return __builtin_amdgcn_ds_read_tr16_b64_v4i16((s16x4 LAS*)p); }
#define MFMA16(a, b, c) __builtin_amdgcn_mfma_f32_16x16x32_bf16((a), (b), (c), 0, 0, 0)
#define MFMA32(a, b, c) __builtin_amdgcn_mfma_f32_32x32x16_bf16((a), (b), (c), 0, 0, 0)
#define WAIT_V(n) asm volatile("s_waitcnt vmcnt(%0)" ::"i"(n) : "memory")
#define BAR() __builtin_amdgcn_s_barrier()

template <int BN> struct BMap;
template <> struct BMap<80> {
  static DI int prow(int k) { return k ^ (((k >> 3) & 1) << 2); }
  static DI int fseg(int k) { return 0; }
};
template <> struct BMap<96> {
  static DI int prow(int k) { return k; }
  static DI int fseg(int k) { return (k >> 3) & 1; }
};
template <> struct BMap<192> {
  static DI int prow(int k) { return k; }
  static DI int fseg(int k) { return ((k >> 1) & 1) | (((k >> 3) & 1) << 1); }
};

template <int BN> DI int boff_lo(int cb, int lane) {
  const int g = lane >> 4, q = (lane >> 2) & 3, p = lane & 3, k = 8 * g + q;
  return BMap<BN>::prow(k) * (2 * BN) + ((cb ^ BMap<BN>::fseg(k)) * 32) + p * 8;
}
template <int BN> DI int boff_hi(int cb, int lane) {
  const int g = lane >> 4, q = (lane >> 2) & 3, p = lane & 3, k = 8 * g + q + 4;
  return BMap<BN>::prow(k) * (2 * BN) + ((cb ^ BMap<BN>::fseg(k)) * 32) + p * 8;
}
DI bf16x8 read_b2(const char LAS* plo, const char LAS* phi) {
  s16x4 lo = tr_read(plo), hi = tr_read(phi);
  return __builtin_shufflevector(lo, hi, 0, 1, 2, 3, 4, 5, 6, 7);
}
DI bf16x8 read_afrag(const char LAS* img, int row, int c) {
  return *(const bf16x8 LAS*)(img + row * 128 + ((c ^ ((row >> 1) & 7)) * 16));
}
#define ST_MODE 0
DI void st8(void* p, u32x2 v) {
  *(u32x2*)p = v;
}
template <int I, int N, class F> DI void sfor(F&& f) {
  if constexpr (I < N) {
    f(std::integral_constant<int, I>{});
    sfor<I + 1, N>(f);
  }
}
template <int OFF> DI void a_tr(s16x4& d, unsigned addr) {
  asm volatile("ds_read_b64_tr_b16 %0, %1 offset:%2" : "=v"(d) : "v"(addr), "i"(OFF));
}
template <int OFF> DI void a_b128(bf16x8& d, unsigned addr) {
  asm volatile("ds_read_b128 %0, %1 offset:%2" : "=v"(d) : "v"(addr), "i"(OFF));
}
template <int N> DI void lgkm_wait() { asm volatile("s_waitcnt lgkmcnt(%0)" ::"i"(N) : "memory"); }
DI void pin(bf16x8& a) { asm volatile("" : "+v"(a)); }
DI void pin(s16x4& a) { asm volatile("" : "+v"(a)); }
DI bf16x8 cat(const s16x4& lo, const s16x4& hi) { return __builtin_shufflevector(lo, hi, 0, 1, 2, 3, 4, 5, 6, 7); }
DI unsigned ldsaddr(char LAS* p) { return (unsigned)(size_t)p; }
DI bf16x8 ones8() {
  bf16x8 o;
#pragma unroll
  for (int j = 0; j < 8; ++j) o[j] = 0x3F80;
  return o;
}
template <int N> DI void wait_stage(int pend) {
  if (pend >= 3) WAIT_V(3 * N);
  else if (pend == 2) WAIT_V(2 * N);
  else if (pend == 1) WAIT_V(N);
  else WAIT_V(0);
}

template <int BN, int NST, int NIT, bool STATS>
struct SmallGemm {
  static constexpr int CPR = BN / 8;
  static constexpr int BPIECES = 128 * BN * 2 / 1024;
  static constexpr int NI = (16 + BPIECES) / 4;
  static constexpr int STAGE = (16 + BPIECES) * 1024;
  static constexpr int NCB = BN / 16;

  static DI void run(char LAS* lds, const u16* A, int lda, int row0, const u16* B, int ldb, int n0, int nmax,
                     int rot, f32x4 (&acc)[NCB], f32x4& rs, f32x4& sq, f32x4& cs0, f32x4& cs1, int kid) {
    const int tid = threadIdx.x, lane = tid & 63;
    const int w = __builtin_amdgcn_readfirstlane(tid >> 6);
    const char* gp[NI];
#pragma unroll
    for (int i = 0; i < NI; ++i) {
      const int pi = i * 4 + w;
      if (i < 4) {
        const int sub = pi >> 3, row = 8 * (pi & 7) + (lane >> 3);
        const int c = (lane & 7) ^ ((row >> 1) & 7);
        gp[i] = (const char*)(A + (size_t)(row0 + row) * lda + sub * 64 + c * 8);
      } else {
        const int ci = (pi - 16) * 64 + lane;
        const int pr = ci / CPR, pc = ci % CPR;
        const int k = BMap<BN>::prow(pr);
        const int c = pc ^ (BMap<BN>::fseg(k) << 1);
        int n = n0 + c * 8;
        n = n > nmax - 8 ? nmax - 8 : n;
        gp[i] = (const char*)(B + (size_t)k * ldb + n);
      }
    }
    const int bstep = 128 * ldb * 2;
    auto issue = [&](int st) {
      char LAS* dst = lds + (st % NST) * STAGE + w * 1024;
#pragma unroll
      for (int i = 0; i < NI; ++i)
        glds16(gp[i] + (size_t)st * (i < 4 ? 256 : bstep), dst + i * 4096);
    };
#pragma unroll
    for (int s = 0; s < NST - 1; ++s) issue(s);
    const int arow = 16 * w + (lane & 15), g = lane >> 4;
    unsigned blo[NCB], bhi[NCB];
#pragma unroll
    for (int p = 0; p < NCB; ++p) {
      int cb = p + rot;
      cb = cb >= NCB ? cb - NCB : cb;
      blo[p] = ldsaddr(lds) + 16384 + boff_lo<BN>(cb, lane);
      bhi[p] = ldsaddr(lds) + 16384 + boff_hi<BN>(cb, lane);
    }
    const unsigned aa0 = ldsaddr(lds) + arow * 128 + ((g ^ ((arow >> 1) & 7)) * 16);
    const unsigned aa1 = ldsaddr(lds) + arow * 128 + (((4 + g) ^ ((arow >> 1) & 7)) * 16);
    const bf16x8 ones = ones8();
    bf16x8 af[2];
    s16x4 bl[2][NCB], bh[2][NCB];
    sfor<0, NIT>([&](auto tc) {
      constexpr int t = decltype(tc)::value;
      constexpr int issued = (t + NST - 1 < NIT) ? (t + NST - 1) : NIT;
      wait_stage<NI>(issued - t - 1);
      BAR();
      if constexpr (t == 0) STAMP(kid, 3);
      if constexpr (t == 2) STAMP(kid, 4);
      if constexpr (t + NST - 1 < NIT) issue(t + NST - 1);
      const unsigned so = (t % NST) * STAGE;
      auto rd = [&](auto kc) {
        constexpr int kk = decltype(kc)::value;
        a_b128<(kk >> 1) * 8192>(af[kk & 1], ((kk & 1) ? aa1 : aa0) + so);
#pragma unroll
        for (int p = 0; p < NCB; ++p) {
          a_tr<kk * 32 * 2 * BN>(bl[kk & 1][p], blo[p] + so);
          a_tr<kk * 32 * 2 * BN>(bh[kk & 1][p], bhi[p] + so);
        }
      };
      rd(std::integral_constant<int, 0>{});
      sfor<0, 4>([&](auto kc) {
        constexpr int kk = decltype(kc)::value;
        if constexpr (kk < 3) {
          rd(std::integral_constant<int, kk + 1>{});
          lgkm_wait<1 + 2 * NCB>();
        } else {
          lgkm_wait<0>();
        }
        pin(af[kk & 1]);
#pragma unroll
        for (int p = 0; p < NCB; ++p) { pin(bl[kk & 1][p]); pin(bh[kk & 1][p]); }
        if (STATS) {
          rs = MFMA16(ones, af[kk & 1], rs);
          sq = MFMA16(af[kk & 1], af[kk & 1], sq);
        }
#pragma unroll
        for (int p = 0; p < NCB; ++p) {
          const bf16x8 bfr = cat(bl[kk & 1][p], bh[kk & 1][p]);
          acc[p] = MFMA16(bfr, af[kk & 1], acc[p]);
          if (STATS && p == 0) cs0 = MFMA16(bfr, ones, cs0);
          if (STATS && p == 4) cs1 = MFMA16(bfr, ones, cs1);
        }
        __builtin_amdgcn_sched_barrier(0);
      });
    });
  }
};

DI void finish_stats(const f32x4& rs, const f32x4& sq, int lane, float& mean, float& rstd) {
  const int m = lane & 15;
  const int r = m & 3;
  float d = r == 0 ? sq[0] : (r == 1 ? sq[1] : (r == 2 ? sq[2] : sq[3]));
  d = __shfl(d, (m >> 2) * 16 + m);
  mean = rs[0] * (1.0f / 640.0f);
  float var = d * (1.0f / 640.0f) - mean * mean;
  var = var < 0.f ? 0.f : var;
  rstd = 1.0f / sqrtf(var + LN_EPS);
}

constexpr int K1_LDS = 3 * 40960 + 1024;
__global__ __launch_bounds__(256, 1) void k1_qkv(const u16* __restrict__ x, const u16* __restrict__ wq,
                                                  const u16* __restrict__ wk, const u16* __restrict__ wv,
                                                  char* __restrict__ ws) {
  extern __shared__ __attribute__((aligned(16))) char smem_[];
  char LAS* lds = (char LAS*)smem_;
  const int tid = threadIdx.x, lane = tid & 63;
  STAMP(0, 0);
  const int w = __builtin_amdgcn_readfirstlane(tid >> 6);
  const int bid = blockIdx.x, xcd = bid & 7, idx = bid >> 3;
  const int rt = xcd * 4 + (idx & 3), ct = idx >> 2;
  const int row0 = rt * 64;
  const int g = lane >> 4, m = lane & 15;
  f32x4 z4 = {0.f, 0.f, 0.f, 0.f};
  f32x4 rs = z4, sq = z4;
  float LAS* cs_l = (float LAS*)(lds + 3 * 40960);
  if (ct < 7) {
    using G = SmallGemm<96, 3, 5, true>;
    f32x4 acc[6], cs0 = z4, cs1 = z4;
#pragma unroll
    for (int p = 0; p < 6; ++p) acc[p] = z4;
    G::run(lds, x, D_, row0, wq, D_, ct * 96, D_, w, acc, rs, sq, cs0, cs1, 0);
    STAMP(0, 2);
    float mean, rstd;
    finish_stats(rs, sq, lane, mean, rstd);
    if (m == 0) {
      *(f32x4 LAS*)(cs_l + w * 16 + 4 * g) = cs0;
      if (w < 2) *(f32x4 LAS*)(cs_l + (4 + w) * 16 + 4 * g) = cs1;
    }
    __syncthreads();
    u16* q = (u16*)(ws + WS_Q);
    const float sc = rstd * QSCALE;
#pragma unroll
    for (int p = 0; p < 6; ++p) {
      int cb = p + w;
      cb = cb >= 6 ? cb - 6 : cb;
      const int n = ct * 96 + cb * 16 + 4 * g;
      const f32x4 c = *(const f32x4 LAS*)(cs_l + cb * 16 + 4 * g);
      if (n < D_) {
        u32x2 o;
        o[0] = pk_bf16((acc[p][0] - mean * c[0]) * sc, (acc[p][1] - mean * c[1]) * sc);
        o[1] = pk_bf16((acc[p][2] - mean * c[2]) * sc, (acc[p][3] - mean * c[3]) * sc);
        st8(q + (size_t)(row0 + 16 * w + m) * D_ + n, o);
      }
    }
  } else {
    constexpr int NI = 7, STAGE = 28 * 1024, NST = 4, NIT = 5;
    const char* gp[NI];
#pragma unroll
    for (int i = 0; i < NI; ++i) {
      const int pi = i * 4 + w;
      if (i < 4) {
        const int sub = pi >> 3, row = 8 * (pi & 7) + (lane >> 3);
        const int c = (lane & 7) ^ ((row >> 1) & 7);
        gp[i] = (const char*)(x + (size_t)(row0 + row) * D_ + sub * 64 + c * 8);
      } else {
        int pj = pi - 16;
        pj = pj >= 10 ? pj - 10 : pj;
        const u16* src = pj < 5 ? wk : wv;
        gp[i] = (const char*)src + (pj < 5 ? pj : pj - 5) * 1024 + lane * 16;
      }
    }
    auto issue = [&](int st) {
      char LAS* dst = lds + (st % NST) * STAGE + w * 1024;
#pragma unroll
      for (int i = 0; i < NI; ++i) glds16(gp[i] + (size_t)st * (i < 4 ? 256 : 5120), dst + i * 4096);
    };
#pragma unroll
    for (int s = 0; s < NST - 1; ++s) issue(s);
    const int arow = 16 * w + m;
    const int q4 = (lane >> 2) & 3, p4 = lane & 3;
    const int bo = (8 * g + q4) * 40 + p4 * 8;
    f32x4 ak0 = z4, ak1 = z4, av0 = z4, av1 = z4, ck0 = z4, ck1 = z4, cv0 = z4, cv1 = z4;
    const bf16x8 ones = ones8();
    const unsigned aa0 = ldsaddr(lds) + arow * 128 + ((g ^ ((arow >> 1) & 7)) * 16);
    const unsigned aa1 = ldsaddr(lds) + arow * 128 + (((4 + g) ^ ((arow >> 1) & 7)) * 16);
    const unsigned bb = ldsaddr(lds) + 16384 + bo;
    sfor<0, NIT>([&](auto tc) {
      constexpr int t = decltype(tc)::value;
      constexpr int issued = (t + NST - 1 < NIT) ? (t + NST - 1) : NIT;
      wait_stage<NI>(issued - t - 1);
      BAR();
      if constexpr (t + NST - 1 < NIT) issue(t + NST - 1);
      const unsigned so = (t % NST) * STAGE;
      sfor<0, 4>([&](auto kc) {
        constexpr int kk = decltype(kc)::value;
        bf16x8 af;
        s16x4 r[8];
        a_b128<(kk >> 1) * 8192>(af, ((kk & 1) ? aa1 : aa0) + so);
        constexpr int o = kk * 32 * 40;
        a_tr<o>(r[0], bb + so); a_tr<o + 160>(r[1], bb + so); a_tr<o + 32>(r[2], bb + so); a_tr<o + 192>(r[3], bb + so);
        a_tr<5120 + o>(r[4], bb + so); a_tr<5120 + o + 160>(r[5], bb + so);
        a_tr<5120 + o + 32>(r[6], bb + so); a_tr<5120 + o + 192>(r[7], bb + so);
        lgkm_wait<0>();
        pin(af);
#pragma unroll
        for (int j = 0; j < 8; ++j) pin(r[j]);
        const bf16x8 k0 = cat(r[0], r[1]), k1 = cat(r[2], r[3]), v0 = cat(r[4], r[5]), v1 = cat(r[6], r[7]);
        rs = MFMA16(ones, af, rs);
        sq = MFMA16(af, af, sq);
        ak0 = MFMA16(k0, af, ak0);
        ak1 = MFMA16(k1, af, ak1);
        ck0 = MFMA16(k0, ones, ck0);
        ck1 = MFMA16(k1, ones, ck1);
        av0 = MFMA16(af, v0, av0);
        av1 = MFMA16(af, v1, av1);
        cv0 = MFMA16(ones, v0, cv0);
        cv1 = MFMA16(ones, v1, cv1);
      });
    });
    float mean, rstd;
    finish_stats(rs, sq, lane, mean, rstd);
    u16* kf = (u16*)(ws + WS_KF);
    u16* vf = (u16*)(ws + WS_VF);
    const int key = row0 + 16 * w + m;
    const int blk = key >> 5, kk5 = key & 31;
    const int r = (kk5 & 19) | ((kk5 & 4) << 1) | ((kk5 & 8) >> 1);
    float kv[4], n2;
    u32x2 o;
#pragma unroll
    for (int j = 0; j < 4; ++j) kv[j] = (ak0[j] - mean * ck0[j]) * rstd;
    o[0] = pk_bf16(kv[0], kv[1]);
    o[1] = pk_bf16(kv[2], kv[3]);
    n2 = bf_lo(o[0]) * bf_lo(o[0]) + bf_hi(o[0]) * bf_hi(o[0]) + bf_lo(o[1]) * bf_lo(o[1]) + bf_hi(o[1]) * bf_hi(o[1]);
    *(u32x2*)(kf + (size_t)blk * 1024 + ((g >> 1) * 32 + r) * 8 + (g & 1) * 4) = o;
    {
      u32x2 o1;
#pragma unroll
      for (int j = 0; j < 4; ++j) kv[j] = (ak1[j] - mean * ck1[j]) * rstd;
      o1[0] = pk_bf16(kv[0], kv[1]);
      o1[1] = pk_bf16(kv[2], kv[3]);
      if (g == 0)
        n2 += bf_lo(o1[0]) * bf_lo(o1[0]) + bf_hi(o1[0]) * bf_hi(o1[0]) + bf_lo(o1[1]) * bf_lo(o1[1]) +
              bf_hi(o1[1]) * bf_hi(o1[1]);
      if (g == 1) { o1[0] = 0x3F803F80u; o1[1] = 0x00003F80u; }
      if (g >= 2) { o1[0] = 0u; o1[1] = 0u; }
      *(u32x2*)(kf + (size_t)blk * 1024 + 512 + ((g >> 1) * 32 + r) * 8 + (g & 1) * 4) = o1;
    }
    n2 += __shfl_xor(n2, 16);
    n2 += __shfl_xor(n2, 32);
#pragma unroll
    for (int s = 1; s < 16; s <<= 1) n2 = fmaxf(n2, __shfl_xor(n2, s));
    float LAS* red = (float LAS*)(lds + 4 * 28 * 1024);
    if (lane == 0) red[w] = n2;
    {
      float mj[4], rj[4];
#pragma unroll
      for (int j = 0; j < 4; ++j) {
        mj[j] = __shfl(mean, 4 * g + j);
        rj[j] = __shfl(rstd, 4 * g + j);
      }
      const int key0 = row0 + 16 * w + 4 * g;
      const int vb = key0 >> 5, k5 = key0 & 31;
      const int s = k5 >> 4, h = (k5 >> 3) & 1, j0 = k5 & 7;
      u16* base = vf + (size_t)vb * 1024 + s * 512 + h * 256 + j0;
      u32x2 ov;
      ov[0] = pk_bf16((av0[0] - mj[0] * cv0[0]) * rj[0], (av0[1] - mj[1] * cv0[1]) * rj[1]);
      ov[1] = pk_bf16((av0[2] - mj[2] * cv0[2]) * rj[2], (av0[3] - mj[3] * cv0[3]) * rj[3]);
      *(u32x2*)(base + m * 8) = ov;
      ov[0] = pk_bf16((av1[0] - mj[0] * cv1[0]) * rj[0], (av1[1] - mj[1] * cv1[1]) * rj[1]);
      ov[1] = pk_bf16((av1[2] - mj[2] * cv1[2]) * rj[2], (av1[3] - mj[3] * cv1[3]) * rj[3]);
      if (m == 4) { ov[0] = 0x3F803F80u; ov[1] = 0x3F803F80u; }
      if (m > 4) { ov[0] = 0u; ov[1] = 0u; }
      *(u32x2*)(base + (16 + m) * 8) = ov;
    }
    __syncthreads();
    if (tid == 0) ((float*)(ws + WS_KMX))[rt] = fmaxf(fmaxf(red[0], red[1]), fmaxf(red[2], red[3]));
  }
  STAMP(0, 1);
}

constexpr int K2_LDS = 4 * 256 * 96 + 4096;
DI void split3(float M, unsigned& s01, unsigned& s2) {
  const float n = -M;
  const unsigned hi = __float_as_uint(n) & 0xffff0000u;
  const float r1 = n - __uint_as_float(hi);
  const unsigned mid = __float_as_uint(r1) & 0xffff0000u;
  const float r2 = r1 - __uint_as_float(mid);
  const unsigned lo = __float_as_uint(r2) & 0xffff0000u;
  s01 = (hi >> 16) | mid;
  s2 = lo >> 16;
}
__global__ __launch_bounds__(512, 2) void k2_attn(const char* __restrict__ wsr, char* __restrict__ ws,
                                                   const char* __restrict__ wo, const char* __restrict__ wg,
                                                   const char* __restrict__ wu, const char* __restrict__ wd) {
  extern __shared__ __attribute__((aligned(16))) char smem_[];
  char LAS* lds = (char LAS*)smem_;
  const int tid = threadIdx.x, lane = tid & 63;
  STAMP(1, 0);
  const int w = __builtin_amdgcn_readfirstlane(tid >> 6);
  const int ch = w & 1, ks = w >> 1;
  const int r = lane & 31, h = lane >> 5;
  const int qrow0 = blockIdx.x * 8;
  const u16* q = (const u16*)(wsr + WS_Q);
  const bf16x8* kf = (const bf16x8*)(wsr + WS_KF) + (size_t)ks * 16 * 128 + lane;
  const bf16x8* vf = (const bf16x8*)(wsr + WS_VF) + (size_t)ks * 16 * 128 + lane;
  bf16x8 q0[4], q1[4];
  float qn[4];
#pragma unroll
  for (int gq = 0; gq < 4; ++gq) {
    const u16* src = q + (size_t)(qrow0 + ch * 4 + gq) * D_ + r * 20;
    const u32x2 a = *(const u32x2*)(src + 8 * h), b = *(const u32x2*)(src + 8 * h + 4);
    u32x2 c = *(const u32x2*)(src + 16);
    if (h) { c[0] = 0u; c[1] = 0u; }
    u32x4 f0 = {a[0], a[1], b[0], b[1]};
    u32x4 f1 = {c[0], c[1], 0u, 0u};
    float n2 = 0.f;
#pragma unroll
    for (int e = 0; e < 4; ++e) n2 += bf_lo(f0[e]) * bf_lo(f0[e]) + bf_hi(f0[e]) * bf_hi(f0[e]);
#pragma unroll
    for (int e = 0; e < 2; ++e) n2 += bf_lo(f1[e]) * bf_lo(f1[e]) + bf_hi(f1[e]) * bf_hi(f1[e]);
    n2 += __shfl_xor(n2, 32);
    qn[gq] = n2;
    q0[gq] = __builtin_bit_cast(bf16x8, f0);
    q1[gq] = __builtin_bit_cast(bf16x8, f1);
  }
  float kmx = ((const float*)(wsr + WS_KMX))[r];
#pragma unroll
  for (int s = 1; s < 32; s <<= 1) kmx = fmaxf(kmx, __shfl_xor(kmx, s));
  float M[4];
  bool unsafe = false;
#pragma unroll
  for (int gq = 0; gq < 4; ++gq) {
    M[gq] = sqrtf(qn[gq] * kmx) * 1.001f + 1e-3f;
    unsafe |= !(M[gq] <= FAST_MAX);
  }
  f32x16 z16;
#pragma unroll
  for (int i = 0; i < 16; ++i) z16[i] = 0.f;
  if (__syncthreads_or(unsafe)) {
    float mx[4] = {-INFINITY, -INFINITY, -INFINITY, -INFINITY};
    for (int b = 0; b < 16; ++b) {
      const bf16x8 k0 = kf[b * 128], k1 = kf[b * 128 + 64];
#pragma unroll
      for (int gq = 0; gq < 4; ++gq) {
        f32x16 s = MFMA32(k0, q0[gq], z16);
        s = MFMA32(k1, q1[gq], s);
#pragma unroll
        for (int i = 0; i < 16; ++i) mx[gq] = fmaxf(mx[gq], s[i]);
      }
    }
    float LAS* mred = (float LAS*)lds;
#pragma unroll
    for (int gq = 0; gq < 4; ++gq) {
      mx[gq] = fmaxf(mx[gq], __shfl_xor(mx[gq], 32));
      if (h == 0) mred[ks * 256 + ch * 128 + gq * 32 + r] = mx[gq];
    }
    __syncthreads();
#pragma unroll
    for (int gq = 0; gq < 4; ++gq) {
      const int c = ch * 128 + gq * 32 + r;
      M[gq] = fmaxf(fmaxf(mred[c], mred[256 + c]), fmaxf(mred[512 + c], mred[768 + c]));
    }
    __syncthreads();
  }
#pragma unroll
  for (int gq = 0; gq < 4; ++gq) {
    unsigned s01, s2;
    split3(M[gq], s01, s2);
    u32x4 f1 = __builtin_bit_cast(u32x4, q1[gq]);
    if (h == 0) { f1[2] = s01; f1[3] = s2; }
    q1[gq] = __builtin_bit_cast(bf16x8, f1);
  }
  STAMP(1, 2);
  f32x16 o[4];
#pragma unroll
  for (int gq = 0; gq < 4; ++gq) o[gq] = z16;
  {
    bf16x8 kA0, kA1, vA0, vA1, kB0, kB1, vB0, vB1;
    f32x16 sA, sB;
    u32x4 p0, p1 = {0u, 0u, 0u, 0u};
    auto ld2 = [&](bf16x8& a0, bf16x8& a1, const bf16x8* p) {
      asm volatile("global_load_dwordx4 %0, %1, off" : "=v"(a0) : "v"(p));
      asm volatile("global_load_dwordx4 %0, %1, off offset:1024" : "=v"(a1) : "v"(p));
    };
#define SB() __builtin_amdgcn_sched_barrier(0)
#define VW2(n, a, b) asm volatile("s_waitcnt vmcnt(" #n ")" : "+v"(a), "+v"(b))
#define EX2(dst, j, sc, i)                                                                               \
    dst[j] = pk_bf16(__builtin_amdgcn_exp2f(sc[i]), __builtin_amdgcn_exp2f(sc[i + 1]));                  \
    dst[j + 1] = pk_bf16(__builtin_amdgcn_exp2f(sc[i + 2]), __builtin_amdgcn_exp2f(sc[i + 3]));
#define SLOT(sc, sn, nk0, nk1, gn, gp, pvv1, gq, cv0, PRE1, POST2, POST3, PRE4) \
    PRE1                                                                        \
    sn = MFMA32(nk0, q0[gn], z16); SB();                                        \
    EX2(p0, 0, sc, 0) SB();                                                     \
    o[gp] = MFMA32(pvv1, __builtin_bit_cast(bf16x8, p1), o[gp]); SB();          \
    POST2                                                                       \
    EX2(p0, 2, sc, 4) SB();                                                     \
    sn = MFMA32(nk1, q1[gn], sn); SB();                                         \
    POST3                                                                       \
    EX2(p1, 0, sc, 8) SB();                                                     \
    PRE4                                                                        \
    o[gq] = MFMA32(cv0, __builtin_bit_cast(bf16x8, p0), o[gq]); SB();           \
    EX2(p1, 2, sc, 12) SB();
#define BLOCK(ck0, ck1, cv0, cv1, nk0, nk1, nv0, nv1, b1, b2)                                             \
    SLOT(sA, sB, ck0, ck1, 1, 3, nv1, 0, cv0, , ld2(nv0, nv1, vf + (b1) * 128);, , VW2(4, cv0, cv1);)     \
    SLOT(sB, sA, ck0, ck1, 2, 0, cv1, 1, cv0, , , , )                                                     \
    SLOT(sA, sB, ck0, ck1, 3, 1, cv1, 2, cv0, , , ld2(ck0, ck1, kf + (b2) * 128);, )                      \
    SLOT(sB, sA, nk0, nk1, 0, 2, cv1, 3, cv0, VW2(4, nk0, nk1);, , , )
    ld2(kA0, kA1, kf);
    ld2(vA0, vA1, vf);
    ld2(kB0, kB1, kf + 128);
#pragma unroll
    for (int j = 0; j < 8; ++j) vB1[j] = 0;
    VW2(4, kA0, kA1);
    sA = MFMA32(kA0, q0[0], z16);
    sA = MFMA32(kA1, q1[0], sA);
    for (int b = 0; b < 16; b += 2) {
      BLOCK(kA0, kA1, vA0, vA1, kB0, kB1, vB0, vB1, b + 1, (b + 2 < 16 ? b + 2 : 15))
      BLOCK(kB0, kB1, vB0, vB1, kA0, kA1, vA0, vA1, (b + 2 < 16 ? b + 2 : 15), (b + 3 < 16 ? b + 3 : 15))
    }
    o[3] = MFMA32(vB1, __builtin_bit_cast(bf16x8, p1), o[3]);
    asm volatile("s_waitcnt vmcnt(0)" ::: "memory");
  }
  STAMP(1, 3);
#define PF_MODE 1
  if (w < 4) {
    constexpr int NSLOT = PF_MODE == 1 ? 1024 : 128, NPC = 3 * 1920 + 800, NIT = (NPC + NSLOT - 1) / NSLOT;
    const int slot = (PF_MODE == 1 ? blockIdx.x : (blockIdx.x >> 3)) * 4 + w;
    unsigned acc_ = 0;
#pragma unroll 8
    for (int i = 0; i < NIT; ++i) {
      int pi = slot + i * NSLOT;
      pi = pi < NPC ? pi : slot;
      const char* base = pi < 1920 ? wd : (pi < 3840 ? wg : (pi < 5760 ? wu : wo));
      const int off = pi < 1920 ? pi : (pi < 3840 ? pi - 1920 : (pi < 5760 ? pi - 3840 : pi - 5760));
      const u32x4 v = *(const u32x4*)(base + (size_t)off * 1024 + lane * 16);
      acc_ ^= v[0];
    }
    asm volatile("" ::"v"(acc_));
  }
  float LAS* red = (float LAS*)lds;
#pragma unroll
  for (int gq = 0; gq < 4; ++gq) {
    float LAS* dst = red + ((size_t)ks * 256 + ch * 128 + gq * 32 + r) * 24 + 4 * h;
#pragma unroll
    for (int t = 0; t < 3; ++t) {
      f32x4 v = {o[gq][4 * t], o[gq][4 * t + 1], o[gq][4 * t + 2], o[gq][4 * t + 3]};
      *(f32x4 LAS*)(dst + 8 * t) = v;
    }
  }
  __syncthreads();
  u16* og = (u16*)(ws + WS_O) + (size_t)qrow0 * D_;
  for (int it = tid; it < 1280; it += 512) {
    const int c = it / 5, qd = it % 5;
    f32x4 a = {0.f, 0.f, 0.f, 0.f};
    float l = 0.f;
#pragma unroll
    for (int s = 0; s < 4; ++s) {
      const float LAS* src = red + ((size_t)s * 256 + c) * 24;
      a += *(const f32x4 LAS*)(src + 4 * qd);
      l += src[20];
    }
    const float inv = 1.0f / l;
    u32x2 ov;
    ov[0] = pk_bf16(a[0] * inv, a[1] * inv);
    ov[1] = pk_bf16(a[2] * inv, a[3] * inv);
    st8(og + (size_t)it * 4, ov);
  }
  STAMP(1, 1);
}

template <int KDIM, bool STATS_OUT>
__global__ __launch_bounds__(256, 1) void k3_proj(const u16* __restrict__ A, const u16* __restrict__ W,
                                                   const u16* __restrict__ res, u16* __restrict__ out,
                                                   float* __restrict__ stats) {
  extern __shared__ __attribute__((aligned(16))) char smem_[];
  char LAS* lds = (char LAS*)smem_;
  const int tid = threadIdx.x, lane = tid & 63;
  STAMP(STATS_OUT ? 2 : 4, 0);
  const int w = __builtin_amdgcn_readfirstlane(tid >> 6);
  const int bid = blockIdx.x, xcd = bid & 7, idx = bid >> 3;
  const int rt = xcd * 4 + (idx & 3), ct = idx >> 2;
  const int g = lane >> 4, m = lane & 15;
  const int row = rt * 64 + 16 * w + m;
  u32x2 rv[5];
#pragma unroll
  for (int p = 0; p < 5; ++p) rv[p] = *(const u32x2*)(res + (size_t)row * D_ + ct * 80 + p * 16 + 4 * g);
  using G = SmallGemm<80, 4, KDIM / 128, false>;
  f32x4 z4 = {0.f, 0.f, 0.f, 0.f};
  f32x4 acc[5], d0 = z4, d1 = z4, d2 = z4, d3 = z4;
#pragma unroll
  for (int p = 0; p < 5; ++p) acc[p] = z4;
  G::run(lds, A, KDIM, rt * 64, W, D_, ct * 80, D_, 0, acc, d0, d1, d2, d3, STATS_OUT ? 2 : 4);
  STAMP(STATS_OUT ? 2 : 4, 2);
  float s1 = 0.f, s2 = 0.f;
#pragma unroll
  for (int p = 0; p < 5; ++p) {
    u32x2 o;
    o[0] = pk_bf16(bf_lo(rv[p][0]) + acc[p][0], bf_hi(rv[p][0]) + acc[p][1]);
    o[1] = pk_bf16(bf_lo(rv[p][1]) + acc[p][2], bf_hi(rv[p][1]) + acc[p][3]);
    if (STATS_OUT) st8(out + (size_t)row * D_ + ct * 80 + p * 16 + 4 * g, o);
    else *(u32x2*)(out + (size_t)row * D_ + ct * 80 + p * 16 + 4 * g) = o;
    if (STATS_OUT) {
      const float a = bf_lo(o[0]), b = bf_hi(o[0]), c = bf_lo(o[1]), d = bf_hi(o[1]);
      s1 += (a + b) + (c + d);
      s2 += (a * a + b * b) + (c * c + d * d);
    }
  }
  if (STATS_OUT) {
    s1 += __shfl_xor(s1, 16); s1 += __shfl_xor(s1, 32);
    s2 += __shfl_xor(s2, 16); s2 += __shfl_xor(s2, 32);
    if (g == 0) { f32x2 v = {s1, s2}; *(f32x2*)(stats + ((size_t)row * 8 + ct) * 2) = v; }
  }
  STAMP(STATS_OUT ? 2 : 4, 1);
}

constexpr int K4_STAGE = 40960, K4_LDS = 3 * K4_STAGE;
DI float gelu_tanh(float x) {
  constexpr float C0 = -2.0f * 0.7978845608028654f * 1.4426950408889634f, C1 = C0 * 0.044715f;
  const float e = __builtin_amdgcn_exp2f(x * __builtin_fmaf(x * x, C1, C0));
  return x * __builtin_amdgcn_rcpf(1.0f + e);
}
__global__ __launch_bounds__(512, 2) void k4_ffn1(const u16* __restrict__ hh, const u16* __restrict__ wg,
                                                   const u16* __restrict__ wu, const float* __restrict__ stats,
                                                   u16* __restrict__ gout) {
  extern __shared__ __attribute__((aligned(16))) char smem_[];
  char LAS* lds = (char LAS*)smem_;
  const int tid = threadIdx.x, lane = tid & 63;
  STAMP(3, 0);
  const int w = __builtin_amdgcn_readfirstlane(tid >> 6);
  const int bid = blockIdx.x, xcd = bid & 7, idx = bid >> 3;
  const int rt = (xcd >> 2) * 8 + (idx & 7), ct = (xcd & 3) * 4 + (idx >> 3);
  const int row0 = rt * 128, f0 = ct * 96;
  const int wr = w >> 1, wc = w & 1;
  const int g = lane >> 4, m = lane & 15;
  constexpr int NI = 5, NST = 3, NIT = 10;
  f32x4 st[2][4];
#pragma unroll
  for (int rb = 0; rb < 2; ++rb)
#pragma unroll
    for (int j = 0; j < 4; ++j)
      st[rb][j] = *(const f32x4*)(stats + ((size_t)(row0 + 32 * wr + 16 * rb + m) * 8 + 2 * j) * 2);
  const int rot = wr >= 3 ? 0 : wr;
  const char* gp[NI];
#pragma unroll
  for (int i = 0; i < NI; ++i) {
    const int pi = i * 8 + w;
    if (i < 2) {
      const int row = 8 * pi + (lane >> 3);
      const int c = (lane & 7) ^ ((row >> 1) & 7);
      gp[i] = (const char*)(hh + (size_t)(row0 + row) * D_ + c * 8);
    } else {
      const int ci = (pi - 16) * 64 + lane;
      const int pr = ci / 24, pc = ci % 24;
      const int c = pc ^ (BMap<192>::fseg(pr) << 1);
      const u16* src = c < 12 ? wg + f0 + c * 8 : wu + f0 + (c - 12) * 8;
      gp[i] = (const char*)(src + (size_t)pr * F_);
    }
  }
  auto issue = [&](int st_) {
    char LAS* dst = lds + (st_ % NST) * K4_STAGE + w * 1024;
#pragma unroll
    for (int i = 0; i < NI; ++i) glds16(gp[i] + (size_t)st_ * (i < 2 ? 128 : 64 * F_ * 2), dst + i * 8192);
  };
#pragma unroll
  for (int s = 0; s < NST - 1; ++s) issue(s);
  unsigned blo[6], bhi[6];
#pragma unroll
  for (int p = 0; p < 6; ++p) {
    int c3 = (p % 3) + rot;
    c3 = c3 >= 3 ? c3 - 3 : c3;
    const int cb = (p < 3 ? 0 : 6) + 3 * wc + c3;
    blo[p] = ldsaddr(lds) + 16384 + boff_lo<192>(cb, lane);
    bhi[p] = ldsaddr(lds) + 16384 + boff_hi<192>(cb, lane);
  }
  const int ar = 32 * wr + m;
  const unsigned aa0 = ldsaddr(lds) + ar * 128 + ((g ^ ((ar >> 1) & 7)) * 16);
  const unsigned aa1 = ldsaddr(lds) + ar * 128 + (((4 + g) ^ ((ar >> 1) & 7)) * 16);
  f32x4 acc[2][6];
#pragma unroll
  for (int rb = 0; rb < 2; ++rb)
#pragma unroll
    for (int p = 0; p < 6; ++p) acc[rb][p] = f32x4{0.f, 0.f, 0.f, 0.f};
  bf16x8 a0[2], a1[2];
  s16x4 bl[2][6], bh[2][6];
  f32x4 cg = {0.f, 0.f, 0.f, 0.f}, cu = {0.f, 0.f, 0.f, 0.f};
  const bf16x8 ones = ones8();
  sfor<0, NIT>([&](auto tc) {
    constexpr int t = decltype(tc)::value;
    constexpr int issued = (t + NST - 1 < NIT) ? (t + NST - 1) : NIT;
    wait_stage<NI>(issued - t - 1);
    BAR();
    if constexpr (t == 0) STAMP(3, 3);
    if constexpr (t == 2) STAMP(3, 4);
    if constexpr (t + NST - 1 < NIT) issue(t + NST - 1);
    const unsigned so = (t % NST) * K4_STAGE;
    auto rd = [&](auto kc) {
      constexpr int kk = decltype(kc)::value;
      a_b128<0>(a0[kk], (kk ? aa1 : aa0) + so);
      a_b128<2048>(a1[kk], (kk ? aa1 : aa0) + so);
#pragma unroll
      for (int p = 0; p < 6; ++p) {
        a_tr<kk * 32 * 384>(bl[kk][p], blo[p] + so);
        a_tr<kk * 32 * 384>(bh[kk][p], bhi[p] + so);
      }
    };
    rd(std::integral_constant<int, 0>{});
    rd(std::integral_constant<int, 1>{});
    sfor<0, 2>([&](auto kc) {
      constexpr int kk = decltype(kc)::value;
      if constexpr (kk == 0) lgkm_wait<14>(); else lgkm_wait<0>();
      pin(a0[kk]); pin(a1[kk]);
#pragma unroll
      for (int p = 0; p < 6; ++p) { pin(bl[kk][p]); pin(bh[kk][p]); }
#pragma unroll
      for (int p = 0; p < 6; ++p) {
        const bf16x8 bfr = cat(bl[kk][p], bh[kk][p]);
        acc[0][p] = MFMA16(bfr, a0[kk], acc[0][p]);
        acc[1][p] = MFMA16(bfr, a1[kk], acc[1][p]);
        if (p == 0) cg = MFMA16(bfr, ones, cg);
        if (p == 3) cu = MFMA16(bfr, ones, cu);
      }
      __builtin_amdgcn_sched_barrier(0);
    });
  });
  STAMP(3, 2);
  float LAS* csl = (float LAS*)(lds + K4_STAGE);
  if (m == 0 && wr < 3) {
    *(f32x4 LAS*)(csl + wc * 96 + rot * 16 + 4 * g) = cg;
    *(f32x4 LAS*)(csl + wc * 96 + 48 + rot * 16 + 4 * g) = cu;
  }
  __syncthreads();
  f32x4 csg[3], csu[3];
#pragma unroll
  for (int p = 0; p < 3; ++p) {
    int c3 = p + rot;
    c3 = c3 >= 3 ? c3 - 3 : c3;
    csg[p] = *(const f32x4 LAS*)(csl + wc * 96 + c3 * 16 + 4 * g);
    csu[p] = *(const f32x4 LAS*)(csl + wc * 96 + 48 + c3 * 16 + 4 * g);
  }
#pragma unroll
  for (int rb = 0; rb < 2; ++rb) {
    const float s1 = (st[rb][0][0] + st[rb][0][2]) + (st[rb][1][0] + st[rb][1][2]) + (st[rb][2][0] + st[rb][2][2]) + (st[rb][3][0] + st[rb][3][2]);
    const float s2 = (st[rb][0][1] + st[rb][0][3]) + (st[rb][1][1] + st[rb][1][3]) + (st[rb][2][1] + st[rb][2][3]) + (st[rb][3][1] + st[rb][3][3]);
    const float mean = s1 * (1.0f / 640.0f);
    float var = s2 * (1.0f / 640.0f) - mean * mean;
    var = var < 0.f ? 0.f : var;
    const float rstd = 1.0f / sqrtf(var + LN_EPS);
    u16* dst = gout + (size_t)(row0 + 32 * wr + 16 * rb + m) * F_ + f0 + 48 * wc + 4 * g;
#pragma unroll
    for (int p = 0; p < 3; ++p) {
      float r4[4];
#pragma unroll
      for (int j = 0; j < 4; ++j) {
        const float ga = (acc[rb][p][j] - mean * csg[p][j]) * rstd;
        const float up = (acc[rb][p + 3][j] - mean * csu[p][j]) * rstd;
        r4[j] = gelu_tanh(ga) * up;
      }
      u32x2 o;
      o[0] = pk_bf16(r4[0], r4[1]);
      o[1] = pk_bf16(r4[2], r4[3]);
      int c3 = p + rot;
      c3 = c3 >= 3 ? c3 - 3 : c3;
      st8(dst + 16 * c3, o);
    }
  }
  STAMP(3, 1);
}

extern "C" void kernel_launch(void* const* d_in, const int* in_sizes, int n_in, void* d_out, int out_size,
                              void* d_ws, size_t ws_size, hipStream_t stream) {
  const u16* x = (const u16*)d_in[0];
  const u16* wq = (const u16*)d_in[1];
  const u16* wk = (const u16*)d_in[2];
  const u16* wv = (const u16*)d_in[3];
  const u16* wo = (const u16*)d_in[4];
  const u16* wg = (const u16*)d_in[6];
  const u16* wu = (const u16*)d_in[7];
  const u16* wd = (const u16*)d_in[8];
  char* ws = (char*)d_ws;
  constexpr int K3_LDS = 4 * 36864;
  hipFuncSetAttribute((const void*)k1_qkv, hipFuncAttributeMaxDynamicSharedMemorySize, K1_LDS);
  hipFuncSetAttribute((const void*)k2_attn, hipFuncAttributeMaxDynamicSharedMemorySize, K2_LDS);
  hipFuncSetAttribute((const void*)k3_proj<640, true>, hipFuncAttributeMaxDynamicSharedMemorySize, K3_LDS);
  hipFuncSetAttribute((const void*)k3_proj<1536, false>, hipFuncAttributeMaxDynamicSharedMemorySize, K3_LDS);
  hipFuncSetAttribute((const void*)k4_ffn1, hipFuncAttributeMaxDynamicSharedMemorySize, K4_LDS);
  k1_qkv<<<256, 256, K1_LDS, stream>>>(x, wq, wk, wv, ws);
  k2_attn<<<256, 512, K2_LDS, stream>>>(ws, ws, (const char*)wo, (const char*)wg, (const char*)wu, (const char*)wd);
  k3_proj<640, true><<<256, 256, K3_LDS, stream>>>((const u16*)(ws + WS_O), wo, x, (u16*)(ws + WS_H),
                                                     (float*)(ws + WS_ST));
  k4_ffn1<<<256, 512, K4_LDS, stream>>>((const u16*)(ws + WS_H), wg, wu, (const float*)(ws + WS_ST),
                                         (u16*)(ws + WS_G));
  k3_proj<1536, false><<<256, 256, K3_LDS, stream>>>((const u16*)(ws + WS_G), wd, (const u16*)(ws + WS_H),
                                                      (u16*)d_out, nullptr);
}
```

```cpp
#include <hip/hip_runtime.h>
#include <hip/hip_bf16.h>
#include <stdint.h>
#include <type_traits>

typedef __attribute__((ext_vector_type(8))) short bf16x8;
typedef __attribute__((ext_vector_type(4))) short s16x4;
typedef __attribute__((ext_vector_type(4))) float f32x4;
typedef __attribute__((ext_vector_type(2))) float f32x2;
typedef __attribute__((ext_vector_type(16))) float f32x16;
typedef __attribute__((ext_vector_type(2))) __bf16 bf16x2_t;
typedef __attribute__((ext_vector_type(4))) unsigned u32x4;
typedef __attribute__((ext_vector_type(2))) unsigned u32x2;
typedef unsigned short u16;
#define LAS __attribute__((address_space(3)))
#define DI __device__ __forceinline__

constexpr int S_ = 2048, D_ = 640, F_ = 1536;
constexpr float QSCALE = 0.22360679774997896f * 1.4426950408889634f;
constexpr float LN_EPS = 1e-5f;
constexpr float FAST_MAX = 40.0f;

constexpr size_t WS_Q = 0;
constexpr size_t WS_KF = WS_Q + 2048 * 640 * 2;
constexpr size_t WS_VF = WS_KF + 64 * 2048;
constexpr size_t WS_KMX = WS_VF + 64 * 2048;
constexpr size_t WS_CS = WS_KMX + 256;
constexpr size_t WS_O = WS_CS + 3072 * 4;
constexpr size_t WS_H = WS_O + 2048 * 640 * 2;
constexpr size_t WS_ST = WS_H + 2048 * 640 * 2;
constexpr size_t WS_G = WS_ST + 2048 * 16 * 4;

#define STAMP(k, i)
DI unsigned pk_bf16(float a, float b) {
  f32x2 v = {a, b};
  return __builtin_bit_cast(unsigned, __builtin_convertvector(v, bf16x2_t));
}
DI float bf_lo(unsigned u) { return __uint_as_float(u << 16); }
DI float bf_hi(unsigned u) { return __uint_as_float(u & 0xffff0000u); }
DI float bfr(float a) { return bf_lo(pk_bf16(a, 0.f)); }
DI void ln_stats(float s1, float s2, float& mean, float& rstd) {
  const float mu = s1 * (1.0f / 640.0f);
  float var = s2 * (1.0f / 640.0f) - mu * mu;
  var = var < 0.f ? 0.f : var;
  mean = bfr(mu);
  rstd = 1.0f / bfr(sqrtf(bfr(bfr(var) + LN_EPS)));
}
DI void glds16(const void* g, char LAS* l) {
  __builtin_amdgcn_global_load_lds((const unsigned*)g, (unsigned LAS*)l, 16, 0, 0);
}
DI s16x4 tr_read(const char LAS* p) { return __builtin_amdgcn_ds_read_tr16_b64_v4i16((s16x4 LAS*)p); }
#define MFMA16(a, b, c) __builtin_amdgcn_mfma_f32_16x16x32_bf16((a), (b), (c), 0, 0, 0)
#define MFMA32(a, b, c) __builtin_amdgcn_mfma_f32_32x32x16_bf16((a), (b), (c), 0, 0, 0)
#define WAIT_V(n) asm volatile("s_waitcnt vmcnt(%0)" ::"i"(n) : "memory")
#define BAR() __builtin_amdgcn_s_barrier()

template <int BN> struct BMap;
template <> struct BMap<80> {
  static DI int prow(int k) { return k ^ (((k >> 3) & 1) << 2); }
  static DI int fseg(int k) { return 0; }
};
template <> struct BMap<96> {
  static DI int prow(int k) { return k; }
  static DI int fseg(int k) { return (k >> 3) & 1; }
};
template <> struct BMap<192> {
  static DI int prow(int k) { return k; }
  static DI int fseg(int k) { return ((k >> 1) & 1) | (((k >> 3) & 1) << 1); }
};

template <int BN> DI int boff_lo(int cb, int lane) {
  const int g = lane >> 4, q = (lane >> 2) & 3, p = lane & 3, k = 8 * g + q;
  return BMap<BN>::prow(k) * (2 * BN) + ((cb ^ BMap<BN>::fseg(k)) * 32) + p * 8;
}
template <int BN> DI int boff_hi(int cb, int lane) {
  const int g = lane >> 4, q = (lane >> 2) & 3, p = lane & 3, k = 8 * g + q + 4;
  return BMap<BN>::prow(k) * (2 * BN) + ((cb ^ BMap<BN>::fseg(k)) * 32) + p * 8;
}
DI bf16x8 read_b2(const char LAS* plo, const char LAS* phi) {
  s16x4 lo = tr_read(plo), hi = tr_read(phi);
  return __builtin_shufflevector(lo, hi, 0, 1, 2, 3, 4, 5, 6, 7);
}
DI bf16x8 read_afrag(const char LAS* img, int row, int c) {
  return *(const bf16x8 LAS*)(img + row * 128 + ((c ^ ((row >> 1) & 7)) * 16));
}
#define ST_MODE 0
DI void st8(void* p, u32x2 v) {
  *(u32x2*)p = v;
}
template <int I, int N, class F> DI void sfor(F&& f) {
  if constexpr (I < N) {
    f(std::integral_constant<int, I>{});
    sfor<I + 1, N>(f);
  }
}
template <int OFF> DI void a_tr(s16x4& d, unsigned addr) {
  asm volatile("ds_read_b64_tr_b16 %0, %1 offset:%2" : "=v"(d) : "v"(addr), "i"(OFF));
}
template <int OFF> DI void a_b128(bf16x8& d, unsigned addr) {
  asm volatile("ds_read_b128 %0, %1 offset:%2" : "=v"(d) : "v"(addr), "i"(OFF));
}
template <int N> DI void lgkm_wait() { asm volatile("s_waitcnt lgkmcnt(%0)" ::"i"(N) : "memory"); }
DI void pin(bf16x8& a) { asm volatile("" : "+v"(a)); }
DI void pin(s16x4& a) { asm volatile("" : "+v"(a)); }
DI bf16x8 cat(const s16x4& lo, const s16x4& hi) { return __builtin_shufflevector(lo, hi, 0, 1, 2, 3, 4, 5, 6, 7); }
DI unsigned ldsaddr(char LAS* p) { return (unsigned)(size_t)p; }
DI bf16x8 ones8() {
  bf16x8 o;
#pragma unroll
  for (int j = 0; j < 8; ++j) o[j] = 0x3F80;
  return o;
}
template <int N> DI void wait_stage(int pend) {
  if (pend >= 3) WAIT_V(3 * N);
  else if (pend == 2) WAIT_V(2 * N);
  else if (pend == 1) WAIT_V(N);
  else WAIT_V(0);
}

template <int BN, int NST, int NIT, bool STATS>
struct SmallGemm {
  static constexpr int CPR = BN / 8;
  static constexpr int BPIECES = 128 * BN * 2 / 1024;
  static constexpr int NI = (16 + BPIECES) / 4;
  static constexpr int STAGE = (16 + BPIECES) * 1024;
  static constexpr int NCB = BN / 16;

  static DI void run(char LAS* lds, const u16* A, int lda, int row0, const u16* B, int ldb, int n0, int nmax,
                     int rot, f32x4 (&acc)[NCB], f32x4& rs, f32x4& sq, f32x4& cs0, f32x4& cs1, int kid) {
    const int tid = threadIdx.x, lane = tid & 63;
    const int w = __builtin_amdgcn_readfirstlane(tid >> 6);
    const char* gp[NI];
#pragma unroll
    for (int i = 0; i < NI; ++i) {
      const int pi = i * 4 + w;
      if (i < 4) {
        const int sub = pi >> 3, row = 8 * (pi & 7) + (lane >> 3);
        const int c = (lane & 7) ^ ((row >> 1) & 7);
        gp[i] = (const char*)(A + (size_t)(row0 + row) * lda + sub * 64 + c * 8);
      } else {
        const int ci = (pi - 16) * 64 + lane;
        const int pr = ci / CPR, pc = ci % CPR;
        const int k = BMap<BN>::prow(pr);
        const int c = pc ^ (BMap<BN>::fseg(k) << 1);
        int n = n0 + c * 8;
        n = n > nmax - 8 ? nmax - 8 : n;
        gp[i] = (const char*)(B + (size_t)k * ldb + n);
      }
    }
    const int bstep = 128 * ldb * 2;
    auto issue = [&](int st) {
      char LAS* dst = lds + (st % NST) * STAGE + w * 1024;
#pragma unroll
      for (int i = 0; i < NI; ++i)
        glds16(gp[i] + (size_t)st * (i < 4 ? 256 : bstep), dst + i * 4096);
    };
#pragma unroll
    for (int s = 0; s < NST - 1; ++s) issue(s);
    const int arow = 16 * w + (lane & 15), g = lane >> 4;
    unsigned blo[NCB], bhi[NCB];
#pragma unroll
    for (int p = 0; p < NCB; ++p) {
      int cb = p + rot;
      cb = cb >= NCB ? cb - NCB : cb;
      blo[p] = ldsaddr(lds) + 16384 + boff_lo<BN>(cb, lane);
      bhi[p] = ldsaddr(lds) + 16384 + boff_hi<BN>(cb, lane);
    }
    const unsigned aa0 = ldsaddr(lds) + arow * 128 + ((g ^ ((arow >> 1) & 7)) * 16);
    const unsigned aa1 = ldsaddr(lds) + arow * 128 + (((4 + g) ^ ((arow >> 1) & 7)) * 16);
    const bf16x8 ones = ones8();
    bf16x8 af[2];
    s16x4 bl[2][NCB], bh[2][NCB];
    sfor<0, NIT>([&](auto tc) {
      constexpr int t = decltype(tc)::value;
      constexpr int issued = (t + NST - 1 < NIT) ? (t + NST - 1) : NIT;
      wait_stage<NI>(issued - t - 1);
      BAR();
      if constexpr (t == 0) STAMP(kid, 3);
      if constexpr (t == 2) STAMP(kid, 4);
      if constexpr (t + NST - 1 < NIT) issue(t + NST - 1);
      const unsigned so = (t % NST) * STAGE;
      auto rd = [&](auto kc) {
        constexpr int kk = decltype(kc)::value;
        a_b128<(kk >> 1) * 8192>(af[kk & 1], ((kk & 1) ? aa1 : aa0) + so);
#pragma unroll
        for (int p = 0; p < NCB; ++p) {
          a_tr<kk * 32 * 2 * BN>(bl[kk & 1][p], blo[p] + so);
          a_tr<kk * 32 * 2 * BN>(bh[kk & 1][p], bhi[p] + so);
        }
      };
      rd(std::integral_constant<int, 0>{});
      sfor<0, 4>([&](auto kc) {
        constexpr int kk = decltype(kc)::value;
        if constexpr (kk < 3) {
          rd(std::integral_constant<int, kk + 1>{});
          lgkm_wait<1 + 2 * NCB>();
        } else {
          lgkm_wait<0>();
        }
        pin(af[kk & 1]);
#pragma unroll
        for (int p = 0; p < NCB; ++p) { pin(bl[kk & 1][p]); pin(bh[kk & 1][p]); }
        if (STATS) {
          rs = MFMA16(ones, af[kk & 1], rs);
          sq = MFMA16(af[kk & 1], af[kk & 1], sq);
        }
#pragma unroll
        for (int p = 0; p < NCB; ++p) {
          const bf16x8 bfr = cat(bl[kk & 1][p], bh[kk & 1][p]);
          acc[p] = MFMA16(bfr, af[kk & 1], acc[p]);
          if (STATS && p == 0) cs0 = MFMA16(bfr, ones, cs0);
          if (STATS && p == 4) cs1 = MFMA16(bfr, ones, cs1);
        }
        __builtin_amdgcn_sched_barrier(0);
      });
    });
  }
};

DI void finish_stats(const f32x4& rs, const f32x4& sq, int lane, float& mean, float& rstd) {
  const int m = lane & 15;
  const int r = m & 3;
  float d = r == 0 ? sq[0] : (r == 1 ? sq[1] : (r == 2 ? sq[2] : sq[3]));
  d = __shfl(d, (m >> 2) * 16 + m);
  ln_stats(rs[0], d, mean, rstd);
}

constexpr int K1_LDS = 3 * 40960 + 1024;
__global__ __launch_bounds__(256, 1) void k1_qkv(const u16* __restrict__ x, const u16* __restrict__ wq,
                                                  const u16* __restrict__ wk, const u16* __restrict__ wv,
                                                  char* __restrict__ ws) {
  extern __shared__ __attribute__((aligned(16))) char smem_[];
  char LAS* lds = (char LAS*)smem_;
  const int tid = threadIdx.x, lane = tid & 63;
  STAMP(0, 0);
  const int w = __builtin_amdgcn_readfirstlane(tid >> 6);
  const int bid = blockIdx.x, xcd = bid & 7, idx = bid >> 3;
  const int rt = xcd * 4 + (idx & 3), ct = idx >> 2;
  const int row0 = rt * 64;
  const int g = lane >> 4, m = lane & 15;
  f32x4 z4 = {0.f, 0.f, 0.f, 0.f};
  f32x4 rs = z4, sq = z4;
  float LAS* cs_l = (float LAS*)(lds + 3 * 40960);
  if (ct < 7) {
    using G = SmallGemm<96, 3, 5, true>;
    f32x4 acc[6], cs0 = z4, cs1 = z4;
#pragma unroll
    for (int p = 0; p < 6; ++p) acc[p] = z4;
    G::run(lds, x, D_, row0, wq, D_, ct * 96, D_, w, acc, rs, sq, cs0, cs1, 0);
    STAMP(0, 2);
    float mean, rstd;
    finish_stats(rs, sq, lane, mean, rstd);
    if (m == 0) {
      *(f32x4 LAS*)(cs_l + w * 16 + 4 * g) = cs0;
      if (w < 2) *(f32x4 LAS*)(cs_l + (4 + w) * 16 + 4 * g) = cs1;
    }
    __syncthreads();
    u16* q = (u16*)(ws + WS_Q);
    const float sc = rstd * QSCALE;
#pragma unroll
    for (int p = 0; p < 6; ++p) {
      int cb = p + w;
      cb = cb >= 6 ? cb - 6 : cb;
      const int n = ct * 96 + cb * 16 + 4 * g;
      const f32x4 c = *(const f32x4 LAS*)(cs_l + cb * 16 + 4 * g);
      if (n < D_) {
        u32x2 o;
        o[0] = pk_bf16((acc[p][0] - mean * c[0]) * sc, (acc[p][1] - mean * c[1]) * sc);
        o[1] = pk_bf16((acc[p][2] - mean * c[2]) * sc, (acc[p][3] - mean * c[3]) * sc);
        st8(q + (size_t)(row0 + 16 * w + m) * D_ + n, o);
      }
    }
  } else {
    constexpr int NI = 7, STAGE = 28 * 1024, NST = 4, NIT = 5;
    const char* gp[NI];
#pragma unroll
    for (int i = 0; i < NI; ++i) {
      const int pi = i * 4 + w;
      if (i < 4) {
        const int sub = pi >> 3, row = 8 * (pi & 7) + (lane >> 3);
        const int c = (lane & 7) ^ ((row >> 1) & 7);
        gp[i] = (const char*)(x + (size_t)(row0 + row) * D_ + sub * 64 + c * 8);
      } else {
        int pj = pi - 16;
        pj = pj >= 10 ? pj - 10 : pj;
        const u16* src = pj < 5 ? wk : wv;
        gp[i] = (const char*)src + (pj < 5 ? pj : pj - 5) * 1024 + lane * 16;
      }
    }
    auto issue = [&](int st) {
      char LAS* dst = lds + (st % NST) * STAGE + w * 1024;
#pragma unroll
      for (int i = 0; i < NI; ++i) glds16(gp[i] + (size_t)st * (i < 4 ? 256 : 5120), dst + i * 4096);
    };
#pragma unroll
    for (int s = 0; s < NST - 1; ++s) issue(s);
    const int arow = 16 * w + m;
    const int q4 = (lane >> 2) & 3, p4 = lane & 3;
    const int bo = (8 * g + q4) * 40 + p4 * 8;
    f32x4 ak0 = z4, ak1 = z4, av0 = z4, av1 = z4, ck0 = z4, ck1 = z4, cv0 = z4, cv1 = z4;
    const bf16x8 ones = ones8();
    const unsigned aa0 = ldsaddr(lds) + arow * 128 + ((g ^ ((arow >> 1) & 7)) * 16);
    const unsigned aa1 = ldsaddr(lds) + arow * 128 + (((4 + g) ^ ((arow >> 1) & 7)) * 16);
    const unsigned bb = ldsaddr(lds) + 16384 + bo;
    sfor<0, NIT>([&](auto tc) {
      constexpr int t = decltype(tc)::value;
      constexpr int issued = (t + NST - 1 < NIT) ? (t + NST - 1) : NIT;
      wait_stage<NI>(issued - t - 1);
      BAR();
      if constexpr (t + NST - 1 < NIT) issue(t + NST - 1);
      const unsigned so = (t % NST) * STAGE;
      sfor<0, 4>([&](auto kc) {
        constexpr int kk = decltype(kc)::value;
        bf16x8 af;
        s16x4 r[8];
        a_b128<(kk >> 1) * 8192>(af, ((kk & 1) ? aa1 : aa0) + so);
        constexpr int o = kk * 32 * 40;
        a_tr<o>(r[0], bb + so); a_tr<o + 160>(r[1], bb + so); a_tr<o + 32>(r[2], bb + so); a_tr<o + 192>(r[3], bb + so);
        a_tr<5120 + o>(r[4], bb + so); a_tr<5120 + o + 160>(r[5], bb + so);
        a_tr<5120 + o + 32>(r[6], bb + so); a_tr<5120 + o + 192>(r[7], bb + so);
        lgkm_wait<0>();
        pin(af);
#pragma unroll
        for (int j = 0; j < 8; ++j) pin(r[j]);
        const bf16x8 k0 = cat(r[0], r[1]), k1 = cat(r[2], r[3]), v0 = cat(r[4], r[5]), v1 = cat(r[6], r[7]);
        rs = MFMA16(ones, af, rs);
        sq = MFMA16(af, af, sq);
        ak0 = MFMA16(k0, af, ak0);
        ak1 = MFMA16(k1, af, ak1);
        ck0 = MFMA16(k0, ones, ck0);
        ck1 = MFMA16(k1, ones, ck1);
        av0 = MFMA16(af, v0, av0);
        av1 = MFMA16(af, v1, av1);
        cv0 = MFMA16(ones, v0, cv0);
        cv1 = MFMA16(ones, v1, cv1);
      });
    });
    float mean, rstd;
    finish_stats(rs, sq, lane, mean, rstd);
    u16* kf = (u16*)(ws + WS_KF);
    u16* vf = (u16*)(ws + WS_VF);
    const int key = row0 + 16 * w + m;
    const int blk = key >> 5, kk5 = key & 31;
    const int r = (kk5 & 19) | ((kk5 & 4) << 1) | ((kk5 & 8) >> 1);
    float kv[4], n2;
    u32x2 o;
#pragma unroll
    for (int j = 0; j < 4; ++j) kv[j] = (ak0[j] - mean * ck0[j]) * rstd;
    o[0] = pk_bf16(kv[0], kv[1]);
    o[1] = pk_bf16(kv[2], kv[3]);
    n2 = bf_lo(o[0]) * bf_lo(o[0]) + bf_hi(o[0]) * bf_hi(o[0]) + bf_lo(o[1]) * bf_lo(o[1]) + bf_hi(o[1]) * bf_hi(o[1]);
    *(u32x2*)(kf + (size_t)blk * 1024 + ((g >> 1) * 32 + r) * 8 + (g & 1) * 4) = o;
    {
      u32x2 o1;
#pragma unroll
      for (int j = 0; j < 4; ++j) kv[j] = (ak1[j] - mean * ck1[j]) * rstd;
      o1[0] = pk_bf16(kv[0], kv[1]);
      o1[1] = pk_bf16(kv[2], kv[3]);
      if (g == 0)
        n2 += bf_lo(o1[0]) * bf_lo(o1[0]) + bf_hi(o1[0]) * bf_hi(o1[0]) + bf_lo(o1[1]) * bf_lo(o1[1]) +
              bf_hi(o1[1]) * bf_hi(o1[1]);
      if (g == 1) { o1[0] = 0x3F803F80u; o1[1] = 0x00003F80u; }
      if (g >= 2) { o1[0] = 0u; o1[1] = 0u; }
      *(u32x2*)(kf + (size_t)blk * 1024 + 512 + ((g >> 1) * 32 + r) * 8 + (g & 1) * 4) = o1;
    }
    n2 += __shfl_xor(n2, 16);
    n2 += __shfl_xor(n2, 32);
#pragma unroll
    for (int s = 1; s < 16; s <<= 1) n2 = fmaxf(n2, __shfl_xor(n2, s));
    float LAS* red = (float LAS*)(lds + 4 * 28 * 1024);
    if (lane == 0) red[w] = n2;
    {
      float mj[4], rj[4];
#pragma unroll
      for (int j = 0; j < 4; ++j) {
        mj[j] = __shfl(mean, 4 * g + j);
        rj[j] = __shfl(rstd, 4 * g + j);
      }
      const int key0 = row0 + 16 * w + 4 * g;
      const int vb = key0 >> 5, k5 = key0 & 31;
      const int s = k5 >> 4, h = (k5 >> 3) & 1, j0 = k5 & 7;
      u16* base = vf + (size_t)vb * 1024 + s * 512 + h * 256 + j0;
      u32x2 ov;
      ov[0] = pk_bf16((av0[0] - mj[0] * cv0[0]) * rj[0], (av0[1] - mj[1] * cv0[1]) * rj[1]);
      ov[1] = pk_bf16((av0[2] - mj[2] * cv0[2]) * rj[2], (av0[3] - mj[3] * cv0[3]) * rj[3]);
      *(u32x2*)(base + m * 8) = ov;
      ov[0] = pk_bf16((av1[0] - mj[0] * cv1[0]) * rj[0], (av1[1] - mj[1] * cv1[1]) * rj[1]);
      ov[1] = pk_bf16((av1[2] - mj[2] * cv1[2]) * rj[2], (av1[3] - mj[3] * cv1[3]) * rj[3]);
      if (m == 4) { ov[0] = 0x3F803F80u; ov[1] = 0x3F803F80u; }
      if (m > 4) { ov[0] = 0u; ov[1] = 0u; }
      *(u32x2*)(base + (16 + m) * 8) = ov;
    }
    __syncthreads();
    if (tid == 0) ((float*)(ws + WS_KMX))[rt] = fmaxf(fmaxf(red[0], red[1]), fmaxf(red[2], red[3]));
  }
  STAMP(0, 1);
}

constexpr int K2_LDS = 4 * 256 * 96 + 4096;
DI void split3(float M, unsigned& s01, unsigned& s2) {
  const float n = -M;
  const unsigned hi = __float_as_uint(n) & 0xffff0000u;
  const float r1 = n - __uint_as_float(hi);
  const unsigned mid = __float_as_uint(r1) & 0xffff0000u;
  const float r2 = r1 - __uint_as_float(mid);
  const unsigned lo = __float_as_uint(r2) & 0xffff0000u;
  s01 = (hi >> 16) | mid;
  s2 = lo >> 16;
}
__global__ __launch_bounds__(512, 2) void k2_attn(const char* __restrict__ wsr, char* __restrict__ ws,
                                                   const char* __restrict__ wo, const char* __restrict__ wg,
                                                   const char* __restrict__ wu, const char* __restrict__ wd) {
  extern __shared__ __attribute__((aligned(16))) char smem_[];
  char LAS* lds = (char LAS*)smem_;
  const int tid = threadIdx.x, lane = tid & 63;
  STAMP(1, 0);
  const int w = __builtin_amdgcn_readfirstlane(tid >> 6);
  const int ch = w & 1, ks = w >> 1;
  const int r = lane & 31, h = lane >> 5;
  const int qrow0 = blockIdx.x * 8;
  const u16* q = (const u16*)(wsr + WS_Q);
  const bf16x8* kf = (const bf16x8*)(wsr + WS_KF) + (size_t)ks * 16 * 128 + lane;
  const bf16x8* vf = (const bf16x8*)(wsr + WS_VF) + (size_t)ks * 16 * 128 + lane;
  bf16x8 q0[4], q1[4];
  float qn[4];
#pragma unroll
  for (int gq = 0; gq < 4; ++gq) {
    const u16* src = q + (size_t)(qrow0 + ch * 4 + gq) * D_ + r * 20;
    const u32x2 a = *(const u32x2*)(src + 8 * h), b = *(const u32x2*)(src + 8 * h + 4);
    u32x2 c = *(const u32x2*)(src + 16);
    if (h) { c[0] = 0u; c[1] = 0u; }
    u32x4 f0 = {a[0], a[1], b[0], b[1]};
    u32x4 f1 = {c[0], c[1], 0u, 0u};
    float n2 = 0.f;
#pragma unroll
    for (int e = 0; e < 4; ++e) n2 += bf_lo(f0[e]) * bf_lo(f0[e]) + bf_hi(f0[e]) * bf_hi(f0[e]);
#pragma unroll
    for (int e = 0; e < 2; ++e) n2 += bf_lo(f1[e]) * bf_lo(f1[e]) + bf_hi(f1[e]) * bf_hi(f1[e]);
    n2 += __shfl_xor(n2, 32);
    qn[gq] = n2;
    q0[gq] = __builtin_bit_cast(bf16x8, f0);
    q1[gq] = __builtin_bit_cast(bf16x8, f1);
  }
  float kmx = ((const float*)(wsr + WS_KMX))[r];
#pragma unroll
  for (int s = 1; s < 32; s <<= 1) kmx = fmaxf(kmx, __shfl_xor(kmx, s));
  float M[4];
  bool unsafe = false;
#pragma unroll
  for (int gq = 0; gq < 4; ++gq) {
    M[gq] = sqrtf(qn[gq] * kmx) * 1.001f + 1e-3f;
    unsafe |= !(M[gq] <= FAST_MAX);
  }
  f32x16 z16;
#pragma unroll
  for (int i = 0; i < 16; ++i) z16[i] = 0.f;
  if (__syncthreads_or(unsafe)) {
    float mx[4] = {-INFINITY, -INFINITY, -INFINITY, -INFINITY};
    for (int b = 0; b < 16; ++b) {
      const bf16x8 k0 = kf[b * 128], k1 = kf[b * 128 + 64];
#pragma unroll
      for (int gq = 0; gq < 4; ++gq) {
        f32x16 s = MFMA32(k0, q0[gq], z16);
        s = MFMA32(k1, q1[gq], s);
#pragma unroll
        for (int i = 0; i < 16; ++i) mx[gq] = fmaxf(mx[gq], s[i]);
      }
    }
    float LAS* mred = (float LAS*)lds;
#pragma unroll
    for (int gq = 0; gq < 4; ++gq) {
      mx[gq] = fmaxf(mx[gq], __shfl_xor(mx[gq], 32));
      if (h == 0) mred[ks * 256 + ch * 128 + gq * 32 + r] = mx[gq];
    }
    __syncthreads();
#pragma unroll
    for (int gq = 0; gq < 4; ++gq) {
      const int c = ch * 128 + gq * 32 + r;
      M[gq] = fmaxf(fmaxf(mred[c], mred[256 + c]), fmaxf(mred[512 + c], mred[768 + c]));
    }
    __syncthreads();
  }
#pragma unroll
  for (int gq = 0; gq < 4; ++gq) {
    unsigned s01, s2;
    split3(M[gq], s01, s2);
    u32x4 f1 = __builtin_bit_cast(u32x4, q1[gq]);
    if (h == 0) { f1[2] = s01; f1[3] = s2; }
    q1[gq] = __builtin_bit_cast(bf16x8, f1);
  }
  STAMP(1, 2);
  f32x16 o[4];
#pragma unroll
  for (int gq = 0; gq < 4; ++gq) o[gq] = z16;
  {
    bf16x8 kA0, kA1, vA0, vA1, kB0, kB1, vB0, vB1;
    f32x16 sA, sB;
    u32x4 p0, p1 = {0u, 0u, 0u, 0u};
    auto ld2 = [&](bf16x8& a0, bf16x8& a1, const bf16x8* p) {
      asm volatile("global_load_dwordx4 %0, %1, off" : "=v"(a0) : "v"(p));
      asm volatile("global_load_dwordx4 %0, %1, off offset:1024" : "=v"(a1) : "v"(p));
    };
#define SB() __builtin_amdgcn_sched_barrier(0)
#define VW2(n, a, b) asm volatile("s_waitcnt vmcnt(" #n ")" : "+v"(a), "+v"(b))
#define EX2(dst, j, sc, i)                                                                               \
    dst[j] = pk_bf16(__builtin_amdgcn_exp2f(sc[i]), __builtin_amdgcn_exp2f(sc[i + 1]));                  \
    dst[j + 1] = pk_bf16(__builtin_amdgcn_exp2f(sc[i + 2]), __builtin_amdgcn_exp2f(sc[i + 3]));
#define SLOT(sc, sn, nk0, nk1, gn, gp, pvv1, gq, cv0, PRE1, POST2, POST3, PRE4) \
    PRE1                                                                        \
    sn = MFMA32(nk0, q0[gn], z16); SB();                                        \
    EX2(p0, 0, sc, 0) SB();                                                     \
    o[gp] = MFMA32(pvv1, __builtin_bit_cast(bf16x8, p1), o[gp]); SB();          \
    POST2                                                                       \
    EX2(p0, 2, sc, 4) SB();                                                     \
    sn = MFMA32(nk1, q1[gn], sn); SB();                                         \
    POST3                                                                       \
    EX2(p1, 0, sc, 8) SB();                                                     \
    PRE4                                                                        \
    o[gq] = MFMA32(cv0, __builtin_bit_cast(bf16x8, p0), o[gq]); SB();           \
    EX2(p1, 2, sc, 12) SB();
#define BLOCK(ck0, ck1, cv0, cv1, nk0, nk1, nv0, nv1, b1, b2)                                             \
    SLOT(sA, sB, ck0, ck1, 1, 3, nv1, 0, cv0, , ld2(nv0, nv1, vf + (b1) * 128);, , VW2(4, cv0, cv1);)     \
    SLOT(sB, sA, ck0, ck1, 2, 0, cv1, 1, cv0, , , , )                                                     \
    SLOT(sA, sB, ck0, ck1, 3, 1, cv1, 2, cv0, , , ld2(ck0, ck1, kf + (b2) * 128);, )                      \
    SLOT(sB, sA, nk0, nk1, 0, 2, cv1, 3, cv0, VW2(4, nk0, nk1);, , , )
    ld2(kA0, kA1, kf);
    ld2(vA0, vA1, vf);
    ld2(kB0, kB1, kf + 128);
#pragma unroll
    for (int j = 0; j < 8; ++j) vB1[j] = 0;
    VW2(4, kA0, kA1);
    sA = MFMA32(kA0, q0[0], z16);
    sA = MFMA32(kA1, q1[0], sA);
    for (int b = 0; b < 16; b += 2) {
      BLOCK(kA0, kA1, vA0, vA1, kB0, kB1, vB0, vB1, b + 1, (b + 2 < 16 ? b + 2 : 15))
      BLOCK(kB0, kB1, vB0, vB1, kA0, kA1, vA0, vA1, (b + 2 < 16 ? b + 2 : 15), (b + 3 < 16 ? b + 3 : 15))
    }
    o[3] = MFMA32(vB1, __builtin_bit_cast(bf16x8, p1), o[3]);
    asm volatile("s_waitcnt vmcnt(0)" ::: "memory");
  }
  STAMP(1, 3);
#define PF_MODE 1
  if (w < 4) {
    constexpr int NSLOT = PF_MODE == 1 ? 1024 : 128, NPC = 3 * 1920 + 800, NIT = (NPC + NSLOT - 1) / NSLOT;
    const int slot = (PF_MODE == 1 ? blockIdx.x : (blockIdx.x >> 3)) * 4 + w;
    unsigned acc_ = 0;
#pragma unroll 8
    for (int i = 0; i < NIT; ++i) {
      int pi = slot + i * NSLOT;
      pi = pi < NPC ? pi : slot;
      const char* base = pi < 1920 ? wd : (pi < 3840 ? wg : (pi < 5760 ? wu : wo));
      const int off = pi < 1920 ? pi : (pi < 3840 ? pi - 1920 : (pi < 5760 ? pi - 3840 : pi - 5760));
      const u32x4 v = *(const u32x4*)(base + (size_t)off * 1024 + lane * 16);
      acc_ ^= v[0];
    }
    asm volatile("" ::"v"(acc_));
  }
  float LAS* red = (float LAS*)lds;
#pragma unroll
  for (int gq = 0; gq < 4; ++gq) {
    float LAS* dst = red + ((size_t)ks * 256 + ch * 128 + gq * 32 + r) * 24 + 4 * h;
#pragma unroll
    for (int t = 0; t < 3; ++t) {
      f32x4 v = {o[gq][4 * t], o[gq][4 * t + 1], o[gq][4 * t + 2], o[gq][4 * t + 3]};
      *(f32x4 LAS*)(dst + 8 * t) = v;
    }
  }
  __syncthreads();
  u16* og = (u16*)(ws + WS_O) + (size_t)qrow0 * D_;
  for (int it = tid; it < 1280; it += 512) {
    const int c = it / 5, qd = it % 5;
    f32x4 a = {0.f, 0.f, 0.f, 0.f};
    float l = 0.f;
#pragma unroll
    for (int s = 0; s < 4; ++s) {
      const float LAS* src = red + ((size_t)s * 256 + c) * 24;
      a += *(const f32x4 LAS*)(src + 4 * qd);
      l += src[20];
    }
    const float inv = 1.0f / l;
    u32x2 ov;
    ov[0] = pk_bf16(a[0] * inv, a[1] * inv);
    ov[1] = pk_bf16(a[2] * inv, a[3] * inv);
    st8(og + (size_t)it * 4, ov);
  }
  STAMP(1, 1);
}

template <int KDIM, bool STATS_OUT>
__global__ __launch_bounds__(256, 1) void k3_proj(const u16* __restrict__ A, const u16* __restrict__ W,
                                                   const u16* __restrict__ res, u16* __restrict__ out,
                                                   float* __restrict__ stats) {
  extern __shared__ __attribute__((aligned(16))) char smem_[];
  char LAS* lds = (char LAS*)smem_;
  const int tid = threadIdx.x, lane = tid & 63;
  STAMP(STATS_OUT ? 2 : 4, 0);
  const int w = __builtin_amdgcn_readfirstlane(tid >> 6);
  const int bid = blockIdx.x, xcd = bid & 7, idx = bid >> 3;
  const int rt = xcd * 4 + (idx & 3), ct = idx >> 2;
  const int g = lane >> 4, m = lane & 15;
  const int row = rt * 64 + 16 * w + m;
  u32x2 rv[5];
#pragma unroll
  for (int p = 0; p < 5; ++p) rv[p] = *(const u32x2*)(res + (size_t)row * D_ + ct * 80 + p * 16 + 4 * g);
  using G = SmallGemm<80, 4, KDIM / 128, false>;
  f32x4 z4 = {0.f, 0.f, 0.f, 0.f};
  f32x4 acc[5], d0 = z4, d1 = z4, d2 = z4, d3 = z4;
#pragma unroll
  for (int p = 0; p < 5; ++p) acc[p] = z4;
  G::run(lds, A, KDIM, rt * 64, W, D_, ct * 80, D_, 0, acc, d0, d1, d2, d3, STATS_OUT ? 2 : 4);
  STAMP(STATS_OUT ? 2 : 4, 2);
  float s1 = 0.f, s2 = 0.f;
#pragma unroll
  for (int p = 0; p < 5; ++p) {
    u32x2 o;
    o[0] = pk_bf16(bf_lo(rv[p][0]) + acc[p][0], bf_hi(rv[p][0]) + acc[p][1]);
    o[1] = pk_bf16(bf_lo(rv[p][1]) + acc[p][2], bf_hi(rv[p][1]) + acc[p][3]);
    if (STATS_OUT) st8(out + (size_t)row * D_ + ct * 80 + p * 16 + 4 * g, o);
    else *(u32x2*)(out + (size_t)row * D_ + ct * 80 + p * 16 + 4 * g) = o;
    if (STATS_OUT) {
      const float a = bf_lo(o[0]), b = bf_hi(o[0]), c = bf_lo(o[1]), d = bf_hi(o[1]);
      s1 += (a + b) + (c + d);
      s2 += (a * a + b * b) + (c * c + d * d);
    }
  }
  if (STATS_OUT) {
    s1 += __shfl_xor(s1, 16); s1 += __shfl_xor(s1, 32);
    s2 += __shfl_xor(s2, 16); s2 += __shfl_xor(s2, 32);
    if (g == 0) { f32x2 v = {s1, s2}; *(f32x2*)(stats + ((size_t)row * 8 + ct) * 2) = v; }
  }
  STAMP(STATS_OUT ? 2 : 4, 1);
}

constexpr int K4_STAGE = 40960, K4_LDS = 3 * K4_STAGE;
DI float gelu_tanh(float x) {
  constexpr float C0 = -2.0f * 0.7978845608028654f * 1.4426950408889634f, C1 = C0 * 0.044715f;
  const float e = __builtin_amdgcn_exp2f(x * __builtin_fmaf(x * x, C1, C0));
  return x * __builtin_amdgcn_rcpf(1.0f + e);
}
__global__ __launch_bounds__(512, 2) void k4_ffn1(const u16* __restrict__ hh, const u16* __restrict__ wg,
                                                   const u16* __restrict__ wu, const float* __restrict__ stats,
                                                   u16* __restrict__ gout) {
  extern __shared__ __attribute__((aligned(16))) char smem_[];
  char LAS* lds = (char LAS*)smem_;
  const int tid = threadIdx.x, lane = tid & 63;
  STAMP(3, 0);
  const int w = __builtin_amdgcn_readfirstlane(tid >> 6);
  const int bid = blockIdx.x, xcd = bid & 7, idx = bid >> 3;
  const int rt = (xcd >> 2) * 8 + (idx & 7), ct = (xcd & 3) * 4 + (idx >> 3);
  const int row0 = rt * 128, f0 = ct * 96;
  const int wr = w >> 1, wc = w & 1;
  const int g = lane >> 4, m = lane & 15;
  constexpr int NI = 5, NST = 3, NIT = 10;
  f32x4 st[2][4];
#pragma unroll
  for (int rb = 0; rb < 2; ++rb)
#pragma unroll
    for (int j = 0; j < 4; ++j)
      st[rb][j] = *(const f32x4*)(stats + ((size_t)(row0 + 32 * wr + 16 * rb + m) * 8 + 2 * j) * 2);
  const int rot = wr >= 3 ? 0 : wr;
  const char* gp[NI];
#pragma unroll
  for (int i = 0; i < NI; ++i) {
    const int pi = i * 8 + w;
    if (i < 2) {
      const int row = 8 * pi + (lane >> 3);
      const int c = (lane & 7) ^ ((row >> 1) & 7);
      gp[i] = (const char*)(hh + (size_t)(row0 + row) * D_ + c * 8);
    } else {
      const int ci = (pi - 16) * 64 + lane;
      const int pr = ci / 24, pc = ci % 24;
      const int c = pc ^ (BMap<192>::fseg(pr) << 1);
      const u16* src = c < 12 ? wg + f0 + c * 8 : wu + f0 + (c - 12) * 8;
      gp[i] = (const char*)(src + (size_t)pr * F_);
    }
  }
  auto issue = [&](int st_) {
    char LAS* dst = lds + (st_ % NST) * K4_STAGE + w * 1024;
#pragma unroll
    for (int i = 0; i < NI; ++i) glds16(gp[i] + (size_t)st_ * (i < 2 ? 128 : 64 * F_ * 2), dst + i * 8192);
  };
#pragma unroll
  for (int s = 0; s < NST - 1; ++s) issue(s);
  unsigned blo[6], bhi[6];
#pragma unroll
  for (int p = 0; p < 6; ++p) {
    int c3 = (p % 3) + rot;
    c3 = c3 >= 3 ? c3 - 3 : c3;
    const int cb = (p < 3 ? 0 : 6) + 3 * wc + c3;
    blo[p] = ldsaddr(lds) + 16384 + boff_lo<192>(cb, lane);
    bhi[p] = ldsaddr(lds) + 16384 + boff_hi<192>(cb, lane);
  }
  const int ar = 32 * wr + m;
  const unsigned aa0 = ldsaddr(lds) + ar * 128 + ((g ^ ((ar >> 1) & 7)) * 16);
  const unsigned aa1 = ldsaddr(lds) + ar * 128 + (((4 + g) ^ ((ar >> 1) & 7)) * 16);
  f32x4 acc[2][6];
#pragma unroll
  for (int rb = 0; rb < 2; ++rb)
#pragma unroll
    for (int p = 0; p < 6; ++p) acc[rb][p] = f32x4{0.f, 0.f, 0.f, 0.f};
  bf16x8 a0[2], a1[2];
  s16x4 bl[2][6], bh[2][6];
  f32x4 cg = {0.f, 0.f, 0.f, 0.f}, cu = {0.f, 0.f, 0.f, 0.f};
  const bf16x8 ones = ones8();
  sfor<0, NIT>([&](auto tc) {
    constexpr int t = decltype(tc)::value;
    constexpr int issued = (t + NST - 1 < NIT) ? (t + NST - 1) : NIT;
    wait_stage<NI>(issued - t - 1);
    BAR();
    if constexpr (t == 0) STAMP(3, 3);
    if constexpr (t == 2) STAMP(3, 4);
    if constexpr (t + NST - 1 < NIT) issue(t + NST - 1);
    const unsigned so = (t % NST) * K4_STAGE;
    auto rd = [&](auto kc) {
      constexpr int kk = decltype(kc)::value;
      a_b128<0>(a0[kk], (kk ? aa1 : aa0) + so);
      a_b128<2048>(a1[kk], (kk ? aa1 : aa0) + so);
#pragma unroll
      for (int p = 0; p < 6; ++p) {
        a_tr<kk * 32 * 384>(bl[kk][p], blo[p] + so);
        a_tr<kk * 32 * 384>(bh[kk][p], bhi[p] + so);
      }
    };
    rd(std::integral_constant<int, 0>{});
    rd(std::integral_constant<int, 1>{});
    sfor<0, 2>([&](auto kc) {
      constexpr int kk = decltype(kc)::value;
      if constexpr (kk == 0) lgkm_wait<14>(); else lgkm_wait<0>();
      pin(a0[kk]); pin(a1[kk]);
#pragma unroll
      for (int p = 0; p < 6; ++p) { pin(bl[kk][p]); pin(bh[kk][p]); }
#pragma unroll
      for (int p = 0; p < 6; ++p) {
        const bf16x8 bfr = cat(bl[kk][p], bh[kk][p]);
        acc[0][p] = MFMA16(bfr, a0[kk], acc[0][p]);
        acc[1][p] = MFMA16(bfr, a1[kk], acc[1][p]);
        if (p == 0) cg = MFMA16(bfr, ones, cg);
        if (p == 3) cu = MFMA16(bfr, ones, cu);
      }
      __builtin_amdgcn_sched_barrier(0);
    });
  });
  STAMP(3, 2);
  float LAS* csl = (float LAS*)(lds + K4_STAGE);
  if (m == 0 && wr < 3) {
    *(f32x4 LAS*)(csl + wc * 96 + rot * 16 + 4 * g) = cg;
    *(f32x4 LAS*)(csl + wc * 96 + 48 + rot * 16 + 4 * g) = cu;
  }
  __syncthreads();
  f32x4 csg[3], csu[3];
#pragma unroll
  for (int p = 0; p < 3; ++p) {
    int c3 = p + rot;
    c3 = c3 >= 3 ? c3 - 3 : c3;
    csg[p] = *(const f32x4 LAS*)(csl + wc * 96 + c3 * 16 + 4 * g);
    csu[p] = *(const f32x4 LAS*)(csl + wc * 96 + 48 + c3 * 16 + 4 * g);
  }
#pragma unroll
  for (int rb = 0; rb < 2; ++rb) {
    const float s1 = (st[rb][0][0] + st[rb][0][2]) + (st[rb][1][0] + st[rb][1][2]) + (st[rb][2][0] + st[rb][2][2]) + (st[rb][3][0] + st[rb][3][2]);
    const float s2 = (st[rb][0][1] + st[rb][0][3]) + (st[rb][1][1] + st[rb][1][3]) + (st[rb][2][1] + st[rb][2][3]) + (st[rb][3][1] + st[rb][3][3]);
    float mean, rstd;
    ln_stats(s1, s2, mean, rstd);
    u16* dst = gout + (size_t)(row0 + 32 * wr + 16 * rb + m) * F_ + f0 + 48 * wc + 4 * g;
#pragma unroll
    for (int p = 0; p < 3; ++p) {
      float r4[4];
#pragma unroll
      for (int j = 0; j < 4; ++j) {
        const float ga = (acc[rb][p][j] - mean * csg[p][j]) * rstd;
        const float up = (acc[rb][p + 3][j] - mean * csu[p][j]) * rstd;
        r4[j] = gelu_tanh(ga) * up;
      }
      u32x2 o;
      o[0] = pk_bf16(r4[0], r4[1]);
      o[1] = pk_bf16(r4[2], r4[3]);
      int c3 = p + rot;
      c3 = c3 >= 3 ? c3 - 3 : c3;
      st8(dst + 16 * c3, o);
    }
  }
  STAMP(3, 1);
}

extern "C" void kernel_launch(void* const* d_in, const int* in_sizes, int n_in, void* d_out, int out_size,
                              void* d_ws, size_t ws_size, hipStream_t stream) {
  const u16* x = (const u16*)d_in[0];
  const u16* wq = (const u16*)d_in[1];
  const u16* wk = (const u16*)d_in[2];
  const u16* wv = (const u16*)d_in[3];
  const u16* wo = (const u16*)d_in[4];
  const u16* wg = (const u16*)d_in[6];
  const u16* wu = (const u16*)d_in[7];
  const u16* wd = (const u16*)d_in[8];
  char* ws = (char*)d_ws;
  constexpr int K3_LDS = 4 * 36864;
  hipFuncSetAttribute((const void*)k1_qkv, hipFuncAttributeMaxDynamicSharedMemorySize, K1_LDS);
  hipFuncSetAttribute((const void*)k2_attn, hipFuncAttributeMaxDynamicSharedMemorySize, K2_LDS);
  hipFuncSetAttribute((const void*)k3_proj<640, true>, hipFuncAttributeMaxDynamicSharedMemorySize, K3_LDS);
  hipFuncSetAttribute((const void*)k3_proj<1536, false>, hipFuncAttributeMaxDynamicSharedMemorySize, K3_LDS);
  hipFuncSetAttribute((const void*)k4_ffn1, hipFuncAttributeMaxDynamicSharedMemorySize, K4_LDS);
  k1_qkv<<<256, 256, K1_LDS, stream>>>(x, wq, wk, wv, ws);
  k2_attn<<<256, 512, K2_LDS, stream>>>(ws, ws, (const char*)wo, (const char*)wg, (const char*)wu, (const char*)wd);
  k3_proj<640, true><<<256, 256, K3_LDS, stream>>>((const u16*)(ws + WS_O), wo, x, (u16*)(ws + WS_H),
                                                     (float*)(ws + WS_ST));
  k4_ffn1<<<256, 512, K4_LDS, stream>>>((const u16*)(ws + WS_H), wg, wu, (const float*)(ws + WS_ST),
                                         (u16*)(ws + WS_G));
  k3_proj<1536, false><<<256, 256, K3_LDS, stream>>>((const u16*)(ws + WS_G), wd, (const u16*)(ws + WS_H),
                                                      (u16*)d_out, nullptr);
}
```

```cpp
#include <hip/hip_runtime.h>
#include <hip/hip_bf16.h>
#include <stdint.h>
#include <type_traits>

typedef __attribute__((ext_vector_type(8))) short bf16x8;
typedef __attribute__((ext_vector_type(4))) short s16x4;
typedef __attribute__((ext_vector_type(4))) float f32x4;
typedef __attribute__((ext_vector_type(2))) float f32x2;
typedef __attribute__((ext_vector_type(16))) float f32x16;
typedef __attribute__((ext_vector_type(2))) __bf16 bf16x2_t;
typedef __attribute__((ext_vector_type(4))) unsigned u32x4;
typedef __attribute__((ext_vector_type(2))) unsigned u32x2;
typedef unsigned short u16;
#define LAS __attribute__((address_space(3)))
#define DI __device__ __forceinline__

constexpr int S_ = 2048, D_ = 640, F_ = 1536;
constexpr float QSCALE = 0.22360679774997896f * 1.4426950408889634f;
constexpr float LN_EPS = 1e-5f;
constexpr float FAST_MAX = 40.0f;

constexpr size_t WS_Q = 0;
constexpr size_t WS_KF = WS_Q + 2048 * 640 * 2;
constexpr size_t WS_VF = WS_KF + 64 * 2048;
constexpr size_t WS_KMX = WS_VF + 64 * 2048;
constexpr size_t WS_CS = WS_KMX + 256;
constexpr size_t WS_O = WS_CS + 3072 * 4;
constexpr size_t WS_H = WS_O + 2048 * 640 * 2;
constexpr size_t WS_ST = WS_H + 2048 * 640 * 2;
constexpr size_t WS_G = WS_ST + 2048 * 16 * 4;
constexpr size_t WS_PK = WS_G + 2048 * 1536 * 2;
constexpr int PK_O = 0, PK_D = 800, PK_GU = 2720, PK_N = 6560;

#define STAMP(k, i)
DI unsigned pk_bf16(float a, float b) {
  f32x2 v = {a, b};
  return __builtin_bit_cast(unsigned, __builtin_convertvector(v, bf16x2_t));
}
DI float bf_lo(unsigned u) { return __uint_as_float(u << 16); }
DI float bf_hi(unsigned u) { return __uint_as_float(u & 0xffff0000u); }
DI float bfr(float a) { return bf_lo(pk_bf16(a, 0.f)); }
DI void ln_stats(float s1, float s2, float& mean, float& rstd) {
  const float mu = s1 * (1.0f / 640.0f);
  float var = s2 * (1.0f / 640.0f) - mu * mu;
  var = var < 0.f ? 0.f : var;
  mean = bfr(mu);
  rstd = 1.0f / bfr(sqrtf(bfr(bfr(var) + LN_EPS)));
}
DI void glds16(const void* g, char LAS* l) {
  __builtin_amdgcn_global_load_lds((const unsigned*)g, (unsigned LAS*)l, 16, 0, 0);
}
DI s16x4 tr_read(const char LAS* p) { return __builtin_amdgcn_ds_read_tr16_b64_v4i16((s16x4 LAS*)p); }
#define MFMA16(a, b, c) __builtin_amdgcn_mfma_f32_16x16x32_bf16((a), (b), (c), 0, 0, 0)
#define MFMA32(a, b, c) __builtin_amdgcn_mfma_f32_32x32x16_bf16((a), (b), (c), 0, 0, 0)
#define WAIT_V(n) asm volatile("s_waitcnt vmcnt(%0)" ::"i"(n) : "memory")
#define BAR() __builtin_amdgcn_s_barrier()

template <int BN> struct BMap;
template <> struct BMap<80> {
  static DI int prow(int k) { return k ^ (((k >> 3) & 1) << 2); }
  static DI int fseg(int k) { return 0; }
};
template <> struct BMap<96> {
  static DI int prow(int k) { return k; }
  static DI int fseg(int k) { return (k >> 3) & 1; }
};
template <> struct BMap<192> {
  static DI int prow(int k) { return k; }
  static DI int fseg(int k) { return ((k >> 1) & 1) | (((k >> 3) & 1) << 1); }
};

template <int BN> DI int boff_lo(int cb, int lane) {
  const int g = lane >> 4, q = (lane >> 2) & 3, p = lane & 3, k = 8 * g + q;
  return BMap<BN>::prow(k) * (2 * BN) + ((cb ^ BMap<BN>::fseg(k)) * 32) + p * 8;
}
template <int BN> DI int boff_hi(int cb, int lane) {
  const int g = lane >> 4, q = (lane >> 2) & 3, p = lane & 3, k = 8 * g + q + 4;
  return BMap<BN>::prow(k) * (2 * BN) + ((cb ^ BMap<BN>::fseg(k)) * 32) + p * 8;
}
DI bf16x8 read_b2(const char LAS* plo, const char LAS* phi) {
  s16x4 lo = tr_read(plo), hi = tr_read(phi);
  return __builtin_shufflevector(lo, hi, 0, 1, 2, 3, 4, 5, 6, 7);
}
DI bf16x8 read_afrag(const char LAS* img, int row, int c) {
  return *(const bf16x8 LAS*)(img + row * 128 + ((c ^ ((row >> 1) & 7)) * 16));
}
#define PK_ST 0
#define ST_MODE 0
DI void st8(void* p, u32x2 v) {
  *(u32x2*)p = v;
}
template <int I, int N, class F> DI void sfor(F&& f) {
  if constexpr (I < N) {
    f(std::integral_constant<int, I>{});
    sfor<I + 1, N>(f);
  }
}
template <int OFF> DI void a_tr(s16x4& d, unsigned addr) {
  asm volatile("ds_read_b64_tr_b16 %0, %1 offset:%2" : "=v"(d) : "v"(addr), "i"(OFF));
}
template <int OFF> DI void a_b128(bf16x8& d, unsigned addr) {
  asm volatile("ds_read_b128 %0, %1 offset:%2" : "=v"(d) : "v"(addr), "i"(OFF));
}
template <int N> DI void lgkm_wait() { asm volatile("s_waitcnt lgkmcnt(%0)" ::"i"(N) : "memory"); }
DI void pin(bf16x8& a) { asm volatile("" : "+v"(a)); }
DI void pin(s16x4& a) { asm volatile("" : "+v"(a)); }
DI bf16x8 cat(const s16x4& lo, const s16x4& hi) { return __builtin_shufflevector(lo, hi, 0, 1, 2, 3, 4, 5, 6, 7); }
DI unsigned ldsaddr(char LAS* p) { return (unsigned)(size_t)p; }
DI bf16x8 ones8() {
  bf16x8 o;
#pragma unroll
  for (int j = 0; j < 8; ++j) o[j] = 0x3F80;
  return o;
}
template <int N> DI void wait_stage(int pend) {
  if (pend >= 3) WAIT_V(3 * N);
  else if (pend == 2) WAIT_V(2 * N);
  else if (pend == 1) WAIT_V(N);
  else WAIT_V(0);
}

template <int BN, int NST, int NIT, bool STATS, bool PACKED>
struct SmallGemm {
  static constexpr int CPR = BN / 8;
  static constexpr int BPIECES = 128 * BN * 2 / 1024;
  static constexpr int NI = (16 + BPIECES) / 4;
  static constexpr int STAGE = (16 + BPIECES) * 1024;
  static constexpr int NCB = BN / 16;

  static DI void run(char LAS* lds, const u16* A, int lda, int row0, const u16* B, int ldb, int n0, int nmax,
                     int rot, f32x4 (&acc)[NCB], f32x4& rs, f32x4& sq, f32x4& cs0, f32x4& cs1, int kid) {
    const int tid = threadIdx.x, lane = tid & 63;
    const int w = __builtin_amdgcn_readfirstlane(tid >> 6);
    const char* gp[NI];
#pragma unroll
    for (int i = 0; i < NI; ++i) {
      const int pi = i * 4 + w;
      if (i < 4) {
        const int sub = pi >> 3, row = 8 * (pi & 7) + (lane >> 3);
        const int c = (lane & 7) ^ ((row >> 1) & 7);
        gp[i] = (const char*)(A + (size_t)(row0 + row) * lda + sub * 64 + c * 8);
      } else if (PACKED) {
        gp[i] = (const char*)B + (pi - 16) * 1024 + lane * 16;
      } else {
        const int ci = (pi - 16) * 64 + lane;
        const int pr = ci / CPR, pc = ci % CPR;
        const int k = BMap<BN>::prow(pr);
        const int c = pc ^ (BMap<BN>::fseg(k) << 1);
        int n = n0 + c * 8;
        n = n > nmax - 8 ? nmax - 8 : n;
        gp[i] = (const char*)(B + (size_t)k * ldb + n);
      }
    }
    const int bstep = PACKED ? BPIECES * 1024 : 128 * ldb * 2;
    auto issue = [&](int st) {
      char LAS* dst = lds + (st % NST) * STAGE + w * 1024;
#pragma unroll
      for (int i = 0; i < NI; ++i)
        glds16(gp[i] + (size_t)st * (i < 4 ? 256 : bstep), dst + i * 4096);
    };
#pragma unroll
    for (int s = 0; s < NST - 1; ++s) issue(s);
    const int arow = 16 * w + (lane & 15), g = lane >> 4;
    unsigned blo[NCB], bhi[NCB];
#pragma unroll
    for (int p = 0; p < NCB; ++p) {
      int cb = p + rot;
      cb = cb >= NCB ? cb - NCB : cb;
      blo[p] = ldsaddr(lds) + 16384 + boff_lo<BN>(cb, lane);
      bhi[p] = ldsaddr(lds) + 16384 + boff_hi<BN>(cb, lane);
    }
    const unsigned aa0 = ldsaddr(lds) + arow * 128 + ((g ^ ((arow >> 1) & 7)) * 16);
    const unsigned aa1 = ldsaddr(lds) + arow * 128 + (((4 + g) ^ ((arow >> 1) & 7)) * 16);
    const bf16x8 ones = ones8();
    bf16x8 af[2];
    s16x4 bl[2][NCB], bh[2][NCB];
    sfor<0, NIT>([&](auto tc) {
      constexpr int t = decltype(tc)::value;
      constexpr int issued = (t + NST - 1 < NIT) ? (t + NST - 1) : NIT;
      wait_stage<NI>(issued - t - 1);
      BAR();
      if constexpr (t == 0) STAMP(kid, 3);
      if constexpr (t == 2) STAMP(kid, 4);
      if constexpr (t + NST - 1 < NIT) issue(t + NST - 1);
      const unsigned so = (t % NST) * STAGE;
      auto rd = [&](auto kc) {
        constexpr int kk = decltype(kc)::value;
        a_b128<(kk >> 1) * 8192>(af[kk & 1], ((kk & 1) ? aa1 : aa0) + so);
#pragma unroll
        for (int p = 0; p < NCB; ++p) {
          a_tr<kk * 32 * 2 * BN>(bl[kk & 1][p], blo[p] + so);
          a_tr<kk * 32 * 2 * BN>(bh[kk & 1][p], bhi[p] + so);
        }
      };
      rd(std::integral_constant<int, 0>{});
      sfor<0, 4>([&](auto kc) {
        constexpr int kk = decltype(kc)::value;
        if constexpr (kk < 3) {
          rd(std::integral_constant<int, kk + 1>{});
          lgkm_wait<1 + 2 * NCB>();
        } else {
          lgkm_wait<0>();
        }
        pin(af[kk & 1]);
#pragma unroll
        for (int p = 0; p < NCB; ++p) { pin(bl[kk & 1][p]); pin(bh[kk & 1][p]); }
        if (STATS) {
          rs = MFMA16(ones, af[kk & 1], rs);
          sq = MFMA16(af[kk & 1], af[kk & 1], sq);
        }
#pragma unroll
        for (int p = 0; p < NCB; ++p) {
          const bf16x8 bfr = cat(bl[kk & 1][p], bh[kk & 1][p]);
          acc[p] = MFMA16(bfr, af[kk & 1], acc[p]);
          if (STATS && p == 0) cs0 = MFMA16(bfr, ones, cs0);
          if (STATS && p == 4) cs1 = MFMA16(bfr, ones, cs1);
        }
        __builtin_amdgcn_sched_barrier(0);
      });
    });
  }
};

DI void finish_stats(const f32x4& rs, const f32x4& sq, int lane, float& mean, float& rstd) {
  const int m = lane & 15;
  const int r = m & 3;
  float d = r == 0 ? sq[0] : (r == 1 ? sq[1] : (r == 2 ? sq[2] : sq[3]));
  d = __shfl(d, (m >> 2) * 16 + m);
  ln_stats(rs[0], d, mean, rstd);
}

constexpr int K1_LDS = 4 * 40960;
__global__ __launch_bounds__(256, 1) void k1_qkv(const u16* __restrict__ x, const u16* __restrict__ wq,
                                                  const u16* __restrict__ wk, const u16* __restrict__ wv,
                                                  char* __restrict__ ws) {
  extern __shared__ __attribute__((aligned(16))) char smem_[];
  char LAS* lds = (char LAS*)smem_;
  const int tid = threadIdx.x, lane = tid & 63;
  STAMP(0, 0);
  const int w = __builtin_amdgcn_readfirstlane(tid >> 6);
  const int bid = blockIdx.x, xcd = bid & 7, idx = bid >> 3;
  const int rt = (xcd >> 1) * 8 + (idx & 7), ct = (xcd & 1) * 4 + (idx >> 3);
  const int row0 = rt * 64;
  const int g = lane >> 4, m = lane & 15;
  f32x4 z4 = {0.f, 0.f, 0.f, 0.f};
  f32x4 rs = z4, sq = z4;
  float LAS* cs_l = (float LAS*)(lds + 2 * 40960);
  if (ct < 7) {
    using G = SmallGemm<96, 4, 5, true, false>;
    f32x4 acc[6], cs0 = z4, cs1 = z4;
#pragma unroll
    for (int p = 0; p < 6; ++p) acc[p] = z4;
    G::run(lds, x, D_, row0, wq, D_, ct * 96, D_, w, acc, rs, sq, cs0, cs1, 0);
    STAMP(0, 2);
    float mean, rstd;
    finish_stats(rs, sq, lane, mean, rstd);
    if (m == 0) {
      *(f32x4 LAS*)(cs_l + w * 16 + 4 * g) = cs0;
      if (w < 2) *(f32x4 LAS*)(cs_l + (4 + w) * 16 + 4 * g) = cs1;
    }
    __syncthreads();
    u16* q = (u16*)(ws + WS_Q);
    const float sc = rstd * QSCALE;
#pragma unroll
    for (int p = 0; p < 6; ++p) {
      int cb = p + w;
      cb = cb >= 6 ? cb - 6 : cb;
      const int n = ct * 96 + cb * 16 + 4 * g;
      const f32x4 c = *(const f32x4 LAS*)(cs_l + cb * 16 + 4 * g);
      if (n < D_) {
        u32x2 o;
        o[0] = pk_bf16((acc[p][0] - mean * c[0]) * sc, (acc[p][1] - mean * c[1]) * sc);
        o[1] = pk_bf16((acc[p][2] - mean * c[2]) * sc, (acc[p][3] - mean * c[3]) * sc);
        st8(q + (size_t)(row0 + 16 * w + m) * D_ + n, o);
      }
    }
  } else {
    constexpr int NI = 7, STAGE = 28 * 1024, NST = 4, NIT = 5;
    const char* gp[NI];
#pragma unroll
    for (int i = 0; i < NI; ++i) {
      const int pi = i * 4 + w;
      if (i < 4) {
        const int sub = pi >> 3, row = 8 * (pi & 7) + (lane >> 3);
        const int c = (lane & 7) ^ ((row >> 1) & 7);
        gp[i] = (const char*)(x + (size_t)(row0 + row) * D_ + sub * 64 + c * 8);
      } else {
        int pj = pi - 16;
        pj = pj >= 10 ? pj - 10 : pj;
        const u16* src = pj < 5 ? wk : wv;
        gp[i] = (const char*)src + (pj < 5 ? pj : pj - 5) * 1024 + lane * 16;
      }
    }
    auto issue = [&](int st) {
      char LAS* dst = lds + (st % NST) * STAGE + w * 1024;
#pragma unroll
      for (int i = 0; i < NI; ++i) glds16(gp[i] + (size_t)st * (i < 4 ? 256 : 5120), dst + i * 4096);
    };
#pragma unroll
    for (int s = 0; s < NST - 1; ++s) issue(s);
    const int arow = 16 * w + m;
    const int q4 = (lane >> 2) & 3, p4 = lane & 3;
    const int bo = (8 * g + q4) * 40 + p4 * 8;
    f32x4 ak0 = z4, ak1 = z4, av0 = z4, av1 = z4, ck0 = z4, ck1 = z4, cv0 = z4, cv1 = z4;
    const bf16x8 ones = ones8();
    const unsigned aa0 = ldsaddr(lds) + arow * 128 + ((g ^ ((arow >> 1) & 7)) * 16);
    const unsigned aa1 = ldsaddr(lds) + arow * 128 + (((4 + g) ^ ((arow >> 1) & 7)) * 16);
    const unsigned bb = ldsaddr(lds) + 16384 + bo;
    sfor<0, NIT>([&](auto tc) {
      constexpr int t = decltype(tc)::value;
      constexpr int issued = (t + NST - 1 < NIT) ? (t + NST - 1) : NIT;
      wait_stage<NI>(issued - t - 1);
      BAR();
      if constexpr (t + NST - 1 < NIT) issue(t + NST - 1);
      const unsigned so = (t % NST) * STAGE;
      sfor<0, 4>([&](auto kc) {
        constexpr int kk = decltype(kc)::value;
        bf16x8 af;
        s16x4 r[8];
        a_b128<(kk >> 1) * 8192>(af, ((kk & 1) ? aa1 : aa0) + so);
        constexpr int o = kk * 32 * 40;
        a_tr<o>(r[0], bb + so); a_tr<o + 160>(r[1], bb + so); a_tr<o + 32>(r[2], bb + so); a_tr<o + 192>(r[3], bb + so);
        a_tr<5120 + o>(r[4], bb + so); a_tr<5120 + o + 160>(r[5], bb + so);
        a_tr<5120 + o + 32>(r[6], bb + so); a_tr<5120 + o + 192>(r[7], bb + so);
        lgkm_wait<0>();
        pin(af);
#pragma unroll
        for (int j = 0; j < 8; ++j) pin(r[j]);
        const bf16x8 k0 = cat(r[0], r[1]), k1 = cat(r[2], r[3]), v0 = cat(r[4], r[5]), v1 = cat(r[6], r[7]);
        rs = MFMA16(ones, af, rs);
        sq = MFMA16(af, af, sq);
        ak0 = MFMA16(k0, af, ak0);
        ak1 = MFMA16(k1, af, ak1);
        ck0 = MFMA16(k0, ones, ck0);
        ck1 = MFMA16(k1, ones, ck1);
        av0 = MFMA16(af, v0, av0);
        av1 = MFMA16(af, v1, av1);
        cv0 = MFMA16(ones, v0, cv0);
        cv1 = MFMA16(ones, v1, cv1);
      });
    });
    float mean, rstd;
    finish_stats(rs, sq, lane, mean, rstd);
    u16* kf = (u16*)(ws + WS_KF);
    u16* vf = (u16*)(ws + WS_VF);
    const int key = row0 + 16 * w + m;
    const int blk = key >> 5, kk5 = key & 31;
    const int r = (kk5 & 19) | ((kk5 & 4) << 1) | ((kk5 & 8) >> 1);
    float kv[4], n2;
    u32x2 o;
#pragma unroll
    for (int j = 0; j < 4; ++j) kv[j] = (ak0[j] - mean * ck0[j]) * rstd;
    o[0] = pk_bf16(kv[0], kv[1]);
    o[1] = pk_bf16(kv[2], kv[3]);
    n2 = bf_lo(o[0]) * bf_lo(o[0]) + bf_hi(o[0]) * bf_hi(o[0]) + bf_lo(o[1]) * bf_lo(o[1]) + bf_hi(o[1]) * bf_hi(o[1]);
    *(u32x2*)(kf + (size_t)blk * 1024 + ((g >> 1) * 32 + r) * 8 + (g & 1) * 4) = o;
    {
      u32x2 o1;
#pragma unroll
      for (int j = 0; j < 4; ++j) kv[j] = (ak1[j] - mean * ck1[j]) * rstd;
      o1[0] = pk_bf16(kv[0], kv[1]);
      o1[1] = pk_bf16(kv[2], kv[3]);
      if (g == 0)
        n2 += bf_lo(o1[0]) * bf_lo(o1[0]) + bf_hi(o1[0]) * bf_hi(o1[0]) + bf_lo(o1[1]) * bf_lo(o1[1]) +
              bf_hi(o1[1]) * bf_hi(o1[1]);
      if (g == 1) { o1[0] = 0x3F803F80u; o1[1] = 0x00003F80u; }
      if (g >= 2) { o1[0] = 0u; o1[1] = 0u; }
      *(u32x2*)(kf + (size_t)blk * 1024 + 512 + ((g >> 1) * 32 + r) * 8 + (g & 1) * 4) = o1;
    }
    n2 += __shfl_xor(n2, 16);
    n2 += __shfl_xor(n2, 32);
#pragma unroll
    for (int s = 1; s < 16; s <<= 1) n2 = fmaxf(n2, __shfl_xor(n2, s));
    float LAS* red = (float LAS*)(lds + 4 * 28 * 1024);
    if (lane == 0) red[w] = n2;
    {
      float mj[4], rj[4];
#pragma unroll
      for (int j = 0; j < 4; ++j) {
        mj[j] = __shfl(mean, 4 * g + j);
        rj[j] = __shfl(rstd, 4 * g + j);
      }
      const int key0 = row0 + 16 * w + 4 * g;
      const int vb = key0 >> 5, k5 = key0 & 31;
      const int s = k5 >> 4, h = (k5 >> 3) & 1, j0 = k5 & 7;
      u16* base = vf + (size_t)vb * 1024 + s * 512 + h * 256 + j0;
      u32x2 ov;
      ov[0] = pk_bf16((av0[0] - mj[0] * cv0[0]) * rj[0], (av0[1] - mj[1] * cv0[1]) * rj[1]);
      ov[1] = pk_bf16((av0[2] - mj[2] * cv0[2]) * rj[2], (av0[3] - mj[3] * cv0[3]) * rj[3]);
      *(u32x2*)(base + m * 8) = ov;
      ov[0] = pk_bf16((av1[0] - mj[0] * cv1[0]) * rj[0], (av1[1] - mj[1] * cv1[1]) * rj[1]);
      ov[1] = pk_bf16((av1[2] - mj[2] * cv1[2]) * rj[2], (av1[3] - mj[3] * cv1[3]) * rj[3]);
      if (m == 4) { ov[0] = 0x3F803F80u; ov[1] = 0x3F803F80u; }
      if (m > 4) { ov[0] = 0u; ov[1] = 0u; }
      *(u32x2*)(base + (16 + m) * 8) = ov;
    }
    __syncthreads();
    if (tid == 0) ((float*)(ws + WS_KMX))[rt] = fmaxf(fmaxf(red[0], red[1]), fmaxf(red[2], red[3]));
  }
  STAMP(0, 1);
}

constexpr int K2_LDS = 4 * 256 * 96 + 4096;
DI void split3(float M, unsigned& s01, unsigned& s2) {
  const float n = -M;
  const unsigned hi = __float_as_uint(n) & 0xffff0000u;
  const float r1 = n - __uint_as_float(hi);
  const unsigned mid = __float_as_uint(r1) & 0xffff0000u;
  const float r2 = r1 - __uint_as_float(mid);
  const unsigned lo = __float_as_uint(r2) & 0xffff0000u;
  s01 = (hi >> 16) | mid;
  s2 = lo >> 16;
}
__global__ __launch_bounds__(512, 2) void k2_attn(const char* __restrict__ wsr, char* __restrict__ ws,
                                                   const char* __restrict__ wo, const char* __restrict__ wg,
                                                   const char* __restrict__ wu, const char* __restrict__ wd) {
  extern __shared__ __attribute__((aligned(16))) char smem_[];
  char LAS* lds = (char LAS*)smem_;
  const int tid = threadIdx.x, lane = tid & 63;
  STAMP(1, 0);
  const int w = __builtin_amdgcn_readfirstlane(tid >> 6);
  const int ch = w & 1, ks = w >> 1;
  const int r = lane & 31, h = lane >> 5;
  const int qrow0 = blockIdx.x * 8;
  const u16* q = (const u16*)(wsr + WS_Q);
  const bf16x8* kf = (const bf16x8*)(wsr + WS_KF) + (size_t)ks * 16 * 128 + lane;
  const bf16x8* vf = (const bf16x8*)(wsr + WS_VF) + (size_t)ks * 16 * 128 + lane;
  bf16x8 kA0, kA1, vA0, vA1, kB0, kB1, vB0, vB1;
  auto ld2 = [&](bf16x8& a0, bf16x8& a1, const bf16x8* p) {
    asm volatile("global_load_dwordx4 %0, %1, off" : "=v"(a0) : "v"(p));
    asm volatile("global_load_dwordx4 %0, %1, off offset:1024" : "=v"(a1) : "v"(p));
  };
  ld2(kA0, kA1, kf);
  ld2(vA0, vA1, vf);
  ld2(kB0, kB1, kf + 128);
  bf16x8 q0[4], q1[4];
  float qn[4];
#pragma unroll
  for (int gq = 0; gq < 4; ++gq) {
    const u16* src = q + (size_t)(qrow0 + ch * 4 + gq) * D_ + r * 20;
    const u32x2 a = *(const u32x2*)(src + 8 * h), b = *(const u32x2*)(src + 8 * h + 4);
    u32x2 c = *(const u32x2*)(src + 16);
    if (h) { c[0] = 0u; c[1] = 0u; }
    u32x4 f0 = {a[0], a[1], b[0], b[1]};
    u32x4 f1 = {c[0], c[1], 0u, 0u};
    float n2 = 0.f;
#pragma unroll
    for (int e = 0; e < 4; ++e) n2 += bf_lo(f0[e]) * bf_lo(f0[e]) + bf_hi(f0[e]) * bf_hi(f0[e]);
#pragma unroll
    for (int e = 0; e < 2; ++e) n2 += bf_lo(f1[e]) * bf_lo(f1[e]) + bf_hi(f1[e]) * bf_hi(f1[e]);
    n2 += __shfl_xor(n2, 32);
    qn[gq] = n2;
    q0[gq] = __builtin_bit_cast(bf16x8, f0);
    q1[gq] = __builtin_bit_cast(bf16x8, f1);
  }
  float kmx = ((const float*)(wsr + WS_KMX))[r];
#pragma unroll
  for (int s = 1; s < 32; s <<= 1) kmx = fmaxf(kmx, __shfl_xor(kmx, s));
  float M[4];
  bool unsafe = false;
#pragma unroll
  for (int gq = 0; gq < 4; ++gq) {
    M[gq] = sqrtf(qn[gq] * kmx) * 1.001f + 1e-3f;
    unsafe |= !(M[gq] <= FAST_MAX);
  }
  f32x16 z16;
#pragma unroll
  for (int i = 0; i < 16; ++i) z16[i] = 0.f;
  if (__syncthreads_or(unsafe)) {
    float mx[4] = {-INFINITY, -INFINITY, -INFINITY, -INFINITY};
    for (int b = 0; b < 16; ++b) {
      const bf16x8 k0 = kf[b * 128], k1 = kf[b * 128 + 64];
#pragma unroll
      for (int gq = 0; gq < 4; ++gq) {
        f32x16 s = MFMA32(k0, q0[gq], z16);
        s = MFMA32(k1, q1[gq], s);
#pragma unroll
        for (int i = 0; i < 16; ++i) mx[gq] = fmaxf(mx[gq], s[i]);
      }
    }
    float LAS* mred = (float LAS*)lds;
#pragma unroll
    for (int gq = 0; gq < 4; ++gq) {
      mx[gq] = fmaxf(mx[gq], __shfl_xor(mx[gq], 32));
      if (h == 0) mred[ks * 256 + ch * 128 + gq * 32 + r] = mx[gq];
    }
    __syncthreads();
#pragma unroll
    for (int gq = 0; gq < 4; ++gq) {
      const int c = ch * 128 + gq * 32 + r;
      M[gq] = fmaxf(fmaxf(mred[c], mred[256 + c]), fmaxf(mred[512 + c], mred[768 + c]));
    }
    __syncthreads();
  }
#pragma unroll
  for (int gq = 0; gq < 4; ++gq) {
    unsigned s01, s2;
    split3(M[gq], s01, s2);
    u32x4 f1 = __builtin_bit_cast(u32x4, q1[gq]);
    if (h == 0) { f1[2] = s01; f1[3] = s2; }
    q1[gq] = __builtin_bit_cast(bf16x8, f1);
  }
  STAMP(1, 2);
  f32x16 o[4];
#pragma unroll
  for (int gq = 0; gq < 4; ++gq) o[gq] = z16;
  {
    f32x16 sA, sB;
    u32x4 p0, p1 = {0u, 0u, 0u, 0u};
#define SB() __builtin_amdgcn_sched_barrier(0)
#define VW2(n, a, b) asm volatile("s_waitcnt vmcnt(" #n ")" : "+v"(a), "+v"(b))
#define EX2(dst, j, sc, i)                                                                               \
    dst[j] = pk_bf16(__builtin_amdgcn_exp2f(sc[i]), __builtin_amdgcn_exp2f(sc[i + 1]));                  \
    dst[j + 1] = pk_bf16(__builtin_amdgcn_exp2f(sc[i + 2]), __builtin_amdgcn_exp2f(sc[i + 3]));
#define SLOT(sc, sn, nk0, nk1, gn, gp, pvv1, gq, cv0, PRE1, POST2, POST3, PRE4) \
    PRE1                                                                        \
    sn = MFMA32(nk0, q0[gn], z16); SB();                                        \
    EX2(p0, 0, sc, 0) SB();                                                     \
    o[gp] = MFMA32(pvv1, __builtin_bit_cast(bf16x8, p1), o[gp]); SB();          \
    POST2                                                                       \
    EX2(p0, 2, sc, 4) SB();                                                     \
    sn = MFMA32(nk1, q1[gn], sn); SB();                                         \
    POST3                                                                       \
    EX2(p1, 0, sc, 8) SB();                                                     \
    PRE4                                                                        \
    o[gq] = MFMA32(cv0, __builtin_bit_cast(bf16x8, p0), o[gq]); SB();           \
    EX2(p1, 2, sc, 12) SB();
#define BLOCK(ck0, ck1, cv0, cv1, nk0, nk1, nv0, nv1, b1, b2)                                             \
    SLOT(sA, sB, ck0, ck1, 1, 3, nv1, 0, cv0, , ld2(nv0, nv1, vf + (b1) * 128);, , VW2(4, cv0, cv1);)     \
    SLOT(sB, sA, ck0, ck1, 2, 0, cv1, 1, cv0, , , , )                                                     \
    SLOT(sA, sB, ck0, ck1, 3, 1, cv1, 2, cv0, , , ld2(ck0, ck1, kf + (b2) * 128);, )                      \
    SLOT(sB, sA, nk0, nk1, 0, 2, cv1, 3, cv0, VW2(4, nk0, nk1);, , , )
#pragma unroll
    for (int j = 0; j < 8; ++j) vB1[j] = 0;
    asm volatile("s_waitcnt vmcnt(0)" : "+v"(kA0), "+v"(kA1), "+v"(vA0), "+v"(vA1), "+v"(kB0), "+v"(kB1));
    sA = MFMA32(kA0, q0[0], z16);
    sA = MFMA32(kA1, q1[0], sA);
    for (int b = 0; b < 16; b += 2) {
      BLOCK(kA0, kA1, vA0, vA1, kB0, kB1, vB0, vB1, b + 1, (b + 2 < 16 ? b + 2 : 15))
      BLOCK(kB0, kB1, vB0, vB1, kA0, kA1, vA0, vA1, (b + 2 < 16 ? b + 2 : 15), (b + 3 < 16 ? b + 3 : 15))
    }
    o[3] = MFMA32(vB1, __builtin_bit_cast(bf16x8, p1), o[3]);
    asm volatile("s_waitcnt vmcnt(0)" ::: "memory");
  }
  STAMP(1, 3);
  if (w < 4) {
    const int slot = blockIdx.x * 4 + w;
    u32x4 v[7];
#pragma unroll
    for (int i = 0; i < 7; ++i) {
      int P = slot + i * 1024;
      P = P < PK_N ? P : slot;
      const u16* src;
      if (P < PK_GU) {
        const bool isd = P >= PK_D;
        const int Pl = isd ? P - PK_D : P;
        const int per = isd ? 240 : 100;
        const int ct = Pl / per, rem = Pl % per, st = rem / 20, pj = rem % 20;
        const int ci = pj * 64 + lane, pr = ci / 10, pc = ci % 10;
        src = (const u16*)(isd ? wd : wo) + (size_t)(st * 128 + BMap<80>::prow(pr)) * D_ + ct * 80 + pc * 8;
      } else {
        const int Pl = P - PK_GU;
        const int ct = Pl / 240, rem = Pl % 240, st = rem / 24, pj = rem % 24;
        const int ci = pj * 64 + lane, pr = ci / 24, pc = ci % 24;
        const int c = pc ^ (BMap<192>::fseg(pr) << 1);
        src = (const u16*)(c < 12 ? wg : wu) + (size_t)(st * 64 + pr) * F_ + ct * 96 + (c < 12 ? c : c - 12) * 8;
      }
      v[i] = *(const u32x4*)src;
    }
#pragma unroll
    for (int i = 0; i < 7; ++i) {
      int P = slot + i * 1024;
      P = P < PK_N ? P : slot;
      u32x4* dst = (u32x4*)(ws + WS_PK + (size_t)P * 1024 + lane * 16);
      *dst = v[i];
    }
  }
  float LAS* red = (float LAS*)lds;
#pragma unroll
  for (int gq = 0; gq < 4; ++gq) {
    float LAS* dst = red + ((size_t)ks * 256 + ch * 128 + gq * 32 + r) * 24 + 4 * h;
#pragma unroll
    for (int t = 0; t < 3; ++t) {
      f32x4 v = {o[gq][4 * t], o[gq][4 * t + 1], o[gq][4 * t + 2], o[gq][4 * t + 3]};
      *(f32x4 LAS*)(dst + 8 * t) = v;
    }
  }
  __syncthreads();
  u16* og = (u16*)(ws + WS_O) + (size_t)qrow0 * D_;
  for (int it = tid; it < 1280; it += 512) {
    const int c = it / 5, qd = it % 5;
    f32x4 a = {0.f, 0.f, 0.f, 0.f};
    float l = 0.f;
#pragma unroll
    for (int s = 0; s < 4; ++s) {
      const float LAS* src = red + ((size_t)s * 256 + c) * 24;
      a += *(const f32x4 LAS*)(src + 4 * qd);
      l += src[20];
    }
    const float inv = 1.0f / l;
    u32x2 ov;
    ov[0] = pk_bf16(a[0] * inv, a[1] * inv);
    ov[1] = pk_bf16(a[2] * inv, a[3] * inv);
    st8(og + (size_t)it * 4, ov);
  }
  STAMP(1, 1);
}

template <int KDIM, bool STATS_OUT>
__global__ __launch_bounds__(256, 1) void k3_proj(const u16* __restrict__ A, const u16* __restrict__ W,
                                                   const u16* __restrict__ res, u16* __restrict__ out,
                                                   float* __restrict__ stats) {
  extern __shared__ __attribute__((aligned(16))) char smem_[];
  char LAS* lds = (char LAS*)smem_;
  const int tid = threadIdx.x, lane = tid & 63;
  STAMP(STATS_OUT ? 2 : 4, 0);
  const int w = __builtin_amdgcn_readfirstlane(tid >> 6);
  const int bid = blockIdx.x, xcd = bid & 7, idx = bid >> 3;
  const int rt = (xcd >> 1) * 8 + (idx & 7), ct = (xcd & 1) * 4 + (idx >> 3);
  const int g = lane >> 4, m = lane & 15;
  const int row = rt * 64 + 16 * w + m;
  u32x2 rv[5];
#pragma unroll
  for (int p = 0; p < 5; ++p) rv[p] = *(const u32x2*)(res + (size_t)row * D_ + ct * 80 + p * 16 + 4 * g);
  using G = SmallGemm<80, 4, KDIM / 128, false, true>;
  f32x4 z4 = {0.f, 0.f, 0.f, 0.f};
  f32x4 acc[5], d0 = z4, d1 = z4, d2 = z4, d3 = z4;
#pragma unroll
  for (int p = 0; p < 5; ++p) acc[p] = z4;
  G::run(lds, A, KDIM, rt * 64, W + (size_t)ct * (KDIM / 128) * 20 * 512, D_, 0, D_, 0, acc, d0, d1, d2, d3, STATS_OUT ? 2 : 4);
  STAMP(STATS_OUT ? 2 : 4, 2);
  float s1 = 0.f, s2 = 0.f;
#pragma unroll
  for (int p = 0; p < 5; ++p) {
    u32x2 o;
    o[0] = pk_bf16(bf_lo(rv[p][0]) + acc[p][0], bf_hi(rv[p][0]) + acc[p][1]);
    o[1] = pk_bf16(bf_lo(rv[p][1]) + acc[p][2], bf_hi(rv[p][1]) + acc[p][3]);
    if (STATS_OUT) st8(out + (size_t)row * D_ + ct * 80 + p * 16 + 4 * g, o);
    else *(u32x2*)(out + (size_t)row * D_ + ct * 80 + p * 16 + 4 * g) = o;
    if (STATS_OUT) {
      const float a = bf_lo(o[0]), b = bf_hi(o[0]), c = bf_lo(o[1]), d = bf_hi(o[1]);
      s1 += (a + b) + (c + d);
      s2 += (a * a + b * b) + (c * c + d * d);
    }
  }
  if (STATS_OUT) {
    s1 += __shfl_xor(s1, 16); s1 += __shfl_xor(s1, 32);
    s2 += __shfl_xor(s2, 16); s2 += __shfl_xor(s2, 32);
    if (g == 0) { f32x2 v = {s1, s2}; *(f32x2*)(stats + ((size_t)row * 8 + ct) * 2) = v; }
  }
  STAMP(STATS_OUT ? 2 : 4, 1);
}

constexpr int K4_STAGE = 40960, K4_LDS = 4 * K4_STAGE;
DI float gelu_tanh(float x) {
  constexpr float C0 = -2.0f * 0.7978845608028654f * 1.4426950408889634f, C1 = C0 * 0.044715f;
  const float e = __builtin_amdgcn_exp2f(x * __builtin_fmaf(x * x, C1, C0));
  return x * __builtin_amdgcn_rcpf(1.0f + e);
}
__global__ __launch_bounds__(512, 2) void k4_ffn1(const u16* __restrict__ hh, const char* __restrict__ wpk,
                                                   const float* __restrict__ stats,
                                                   u16* __restrict__ gout) {
  extern __shared__ __attribute__((aligned(16))) char smem_[];
  char LAS* lds = (char LAS*)smem_;
  const int tid = threadIdx.x, lane = tid & 63;
  STAMP(3, 0);
  const int w = __builtin_amdgcn_readfirstlane(tid >> 6);
  const int bid = blockIdx.x, xcd = bid & 7, idx = bid >> 3;
  const int rt = (xcd >> 2) * 8 + (idx & 7), ct = (xcd & 3) * 4 + (idx >> 3);
  const int row0 = rt * 128, f0 = ct * 96;
  const int wr = w >> 1, wc = w & 1;
  const int g = lane >> 4, m = lane & 15;
  constexpr int NI = 5, NST = 4, NIT = 10;
  f32x4 st[2][4];
#pragma unroll
  for (int rb = 0; rb < 2; ++rb)
#pragma unroll
    for (int j = 0; j < 4; ++j)
      st[rb][j] = *(const f32x4*)(stats + ((size_t)(row0 + 32 * wr + 16 * rb + m) * 8 + 2 * j) * 2);
  const int rot = wr >= 3 ? 0 : wr;
  const char* gp[NI];
#pragma unroll
  for (int i = 0; i < NI; ++i) {
    const int pi = i * 8 + w;
    if (i < 2) {
      const int row = 8 * pi + (lane >> 3);
      const int c = (lane & 7) ^ ((row >> 1) & 7);
      gp[i] = (const char*)(hh + (size_t)(row0 + row) * D_ + c * 8);
    } else {
      gp[i] = wpk + (size_t)ct * 240 * 1024 + (pi - 16) * 1024 + lane * 16;
    }
  }
  auto issue = [&](int st_) {
    char LAS* dst = lds + (st_ % NST) * K4_STAGE + w * 1024;
#pragma unroll
    for (int i = 0; i < NI; ++i) glds16(gp[i] + (size_t)st_ * (i < 2 ? 128 : 24 * 1024), dst + i * 8192);
  };
#pragma unroll
  for (int s = 0; s < NST - 1; ++s) issue(s);
  unsigned blo[6], bhi[6];
#pragma unroll
  for (int p = 0; p < 6; ++p) {
    int c3 = (p % 3) + rot;
    c3 = c3 >= 3 ? c3 - 3 : c3;
    const int cb = (p < 3 ? 0 : 6) + 3 * wc + c3;
    blo[p] = ldsaddr(lds) + 16384 + boff_lo<192>(cb, lane);
    bhi[p] = ldsaddr(lds) + 16384 + boff_hi<192>(cb, lane);
  }
  const int ar = 32 * wr + m;
  const unsigned aa0 = ldsaddr(lds) + ar * 128 + ((g ^ ((ar >> 1) & 7)) * 16);
  const unsigned aa1 = ldsaddr(lds) + ar * 128 + (((4 + g) ^ ((ar >> 1) & 7)) * 16);
  f32x4 acc[2][6];
#pragma unroll
  for (int rb = 0; rb < 2; ++rb)
#pragma unroll
    for (int p = 0; p < 6; ++p) acc[rb][p] = f32x4{0.f, 0.f, 0.f, 0.f};
  bf16x8 a0[2], a1[2];
  s16x4 bl[2][6], bh[2][6];
  f32x4 cg = {0.f, 0.f, 0.f, 0.f}, cu = {0.f, 0.f, 0.f, 0.f};
  const bf16x8 ones = ones8();
  sfor<0, NIT>([&](auto tc) {
    constexpr int t = decltype(tc)::value;
    constexpr int issued = (t + NST - 1 < NIT) ? (t + NST - 1) : NIT;
    wait_stage<NI>(issued - t - 1);
    BAR();
    if constexpr (t == 0) STAMP(3, 3);
    if constexpr (t == 2) STAMP(3, 4);
    if constexpr (t + NST - 1 < NIT) issue(t + NST - 1);
    const unsigned so = (t % NST) * K4_STAGE;
    auto rd = [&](auto kc) {
      constexpr int kk = decltype(kc)::value;
      a_b128<0>(a0[kk], (kk ? aa1 : aa0) + so);
      a_b128<2048>(a1[kk], (kk ? aa1 : aa0) + so);
#pragma unroll
      for (int p = 0; p < 6; ++p) {
        a_tr<kk * 32 * 384>(bl[kk][p], blo[p] + so);
        a_tr<kk * 32 * 384>(bh[kk][p], bhi[p] + so);
      }
    };
    rd(std::integral_constant<int, 0>{});
    rd(std::integral_constant<int, 1>{});
    sfor<0, 2>([&](auto kc) {
      constexpr int kk = decltype(kc)::value;
      if constexpr (kk == 0) lgkm_wait<14>(); else lgkm_wait<0>();
      pin(a0[kk]); pin(a1[kk]);
#pragma unroll
      for (int p = 0; p < 6; ++p) { pin(bl[kk][p]); pin(bh[kk][p]); }
#pragma unroll
      for (int p = 0; p < 6; ++p) {
        const bf16x8 bfr = cat(bl[kk][p], bh[kk][p]);
        acc[0][p] = MFMA16(bfr, a0[kk], acc[0][p]);
        acc[1][p] = MFMA16(bfr, a1[kk], acc[1][p]);
        if (p == 0) cg = MFMA16(bfr, ones, cg);
        if (p == 3) cu = MFMA16(bfr, ones, cu);
      }
      __builtin_amdgcn_sched_barrier(0);
    });
  });
  STAMP(3, 2);
  float LAS* csl = (float LAS*)(lds + 2 * K4_STAGE);
  if (m == 0 && wr < 3) {
    *(f32x4 LAS*)(csl + wc * 96 + rot * 16 + 4 * g) = cg;
    *(f32x4 LAS*)(csl + wc * 96 + 48 + rot * 16 + 4 * g) = cu;
  }
  __syncthreads();
  f32x4 csg[3], csu[3];
#pragma unroll
  for (int p = 0; p < 3; ++p) {
    int c3 = p + rot;
    c3 = c3 >= 3 ? c3 - 3 : c3;
    csg[p] = *(const f32x4 LAS*)(csl + wc * 96 + c3 * 16 + 4 * g);
    csu[p] = *(const f32x4 LAS*)(csl + wc * 96 + 48 + c3 * 16 + 4 * g);
  }
#pragma unroll
  for (int rb = 0; rb < 2; ++rb) {
    const float s1 = (st[rb][0][0] + st[rb][0][2]) + (st[rb][1][0] + st[rb][1][2]) + (st[rb][2][0] + st[rb][2][2]) + (st[rb][3][0] + st[rb][3][2]);
    const float s2 = (st[rb][0][1] + st[rb][0][3]) + (st[rb][1][1] + st[rb][1][3]) + (st[rb][2][1] + st[rb][2][3]) + (st[rb][3][1] + st[rb][3][3]);
    float mean, rstd;
    ln_stats(s1, s2, mean, rstd);
    u16* dst = gout + (size_t)(row0 + 32 * wr + 16 * rb + m) * F_ + f0 + 48 * wc + 4 * g;
#pragma unroll
    for (int p = 0; p < 3; ++p) {
      float r4[4];
#pragma unroll
      for (int j = 0; j < 4; ++j) {
        const float ga = (acc[rb][p][j] - mean * csg[p][j]) * rstd;
        const float up = (acc[rb][p + 3][j] - mean * csu[p][j]) * rstd;
        r4[j] = gelu_tanh(ga) * up;
      }
      u32x2 o;
      o[0] = pk_bf16(r4[0], r4[1]);
      o[1] = pk_bf16(r4[2], r4[3]);
      int c3 = p + rot;
      c3 = c3 >= 3 ? c3 - 3 : c3;
      st8(dst + 16 * c3, o);
    }
  }
  STAMP(3, 1);
}

extern "C" void kernel_launch(void* const* d_in, const int* in_sizes, int n_in, void* d_out, int out_size,
                              void* d_ws, size_t ws_size, hipStream_t stream) {
  const u16* x = (const u16*)d_in[0];
  const u16* wq = (const u16*)d_in[1];
  const u16* wk = (const u16*)d_in[2];
  const u16* wv = (const u16*)d_in[3];
  const u16* wo = (const u16*)d_in[4];
  const u16* wg = (const u16*)d_in[6];
  const u16* wu = (const u16*)d_in[7];
  const u16* wd = (const u16*)d_in[8];
  char* ws = (char*)d_ws;
  constexpr int K3_LDS = 4 * 36864;
  hipFuncSetAttribute((const void*)k1_qkv, hipFuncAttributeMaxDynamicSharedMemorySize, K1_LDS);
  hipFuncSetAttribute((const void*)k2_attn, hipFuncAttributeMaxDynamicSharedMemorySize, K2_LDS);
  hipFuncSetAttribute((const void*)k3_proj<640, true>, hipFuncAttributeMaxDynamicSharedMemorySize, K3_LDS);
  hipFuncSetAttribute((const void*)k3_proj<1536, false>, hipFuncAttributeMaxDynamicSharedMemorySize, K3_LDS);
  hipFuncSetAttribute((const void*)k4_ffn1, hipFuncAttributeMaxDynamicSharedMemorySize, K4_LDS);
  k1_qkv<<<256, 256, K1_LDS, stream>>>(x, wq, wk, wv, ws);
  k2_attn<<<256, 512, K2_LDS, stream>>>(ws, ws, (const char*)wo, (const char*)wg, (const char*)wu, (const char*)wd);
  k3_proj<640, true><<<256, 256, K3_LDS, stream>>>((const u16*)(ws + WS_O), (const u16*)(ws + WS_PK), x, (u16*)(ws + WS_H),
                                                     (float*)(ws + WS_ST));
  k4_ffn1<<<256, 512, K4_LDS, stream>>>((const u16*)(ws + WS_H), ws + WS_PK + (size_t)PK_GU * 1024, (const float*)(ws + WS_ST),
                                         (u16*)(ws + WS_G));
  k3_proj<1536, false><<<256, 256, K3_LDS, stream>>>((const u16*)(ws + WS_G), (const u16*)(ws + WS_PK + (size_t)PK_D * 1024), (const u16*)(ws + WS_H),
                                                      (u16*)d_out, nullptr);
}
```

```cpp
#include <hip/hip_runtime.h>
#include <hip/hip_bf16.h>
#include <stdint.h>
#include <type_traits>

typedef __attribute__((ext_vector_type(8))) short bf16x8;
typedef __attribute__((ext_vector_type(4))) short s16x4;
typedef __attribute__((ext_vector_type(4))) float f32x4;
typedef __attribute__((ext_vector_type(2))) float f32x2;
typedef __attribute__((ext_vector_type(16))) float f32x16;
typedef __attribute__((ext_vector_type(2))) __bf16 bf16x2_t;
typedef __attribute__((ext_vector_type(4))) unsigned u32x4;
typedef __attribute__((ext_vector_type(2))) unsigned u32x2;
typedef unsigned short u16;
#define LAS __attribute__((address_space(3)))
#define DI __device__ __forceinline__

constexpr int S_ = 2048, D_ = 640, F_ = 1536;
constexpr float QSCALE = 0.22360679774997896f * 1.4426950408889634f;
constexpr float LN_EPS = 1e-5f;
constexpr float FAST_MAX = 40.0f;

constexpr size_t WS_Q = 0;
constexpr size_t WS_KF = WS_Q + 2048 * 640 * 2;
constexpr size_t WS_VF = WS_KF + 64 * 2048;
constexpr size_t WS_KMX = WS_VF + 64 * 2048;
constexpr size_t WS_CS = WS_KMX + 256;
constexpr size_t WS_O = WS_CS + 3072 * 4;
constexpr size_t WS_H = WS_O + 2048 * 640 * 2;
constexpr size_t WS_ST = WS_H + 2048 * 640 * 2;
constexpr size_t WS_G = WS_ST + 2048 * 16 * 4;

DI unsigned pk_bf16(float a, float b) {
  f32x2 v = {a, b};
  return __builtin_bit_cast(unsigned, __builtin_convertvector(v, bf16x2_t));
}
DI float bf_lo(unsigned u) { return __uint_as_float(u << 16); }
DI float bf_hi(unsigned u) { return __uint_as_float(u & 0xffff0000u); }
DI float bfr(float a) { return bf_lo(pk_bf16(a, 0.f)); }
DI void ln_stats(float s1, float s2, float& mean, float& rstd) {
  const float mu = s1 * (1.0f / 640.0f);
  float var = s2 * (1.0f / 640.0f) - mu * mu;
  var = var < 0.f ? 0.f : var;
  mean = bfr(mu);
  rstd = 1.0f / bfr(sqrtf(bfr(bfr(var) + LN_EPS)));
}
DI void glds16(const void* g, char LAS* l) {
  __builtin_amdgcn_global_load_lds((const unsigned*)g, (unsigned LAS*)l, 16, 0, 0);
}
DI s16x4 tr_read(const char LAS* p) { return __builtin_amdgcn_ds_read_tr16_b64_v4i16((s16x4 LAS*)p); }
#define MFMA16(a, b, c) __builtin_amdgcn_mfma_f32_16x16x32_bf16((a), (b), (c), 0, 0, 0)
#define MFMA32(a, b, c) __builtin_amdgcn_mfma_f32_32x32x16_bf16((a), (b), (c), 0, 0, 0)
#define WAIT_V(n) asm volatile("s_waitcnt vmcnt(%0)" ::"i"(n) : "memory")
#define BAR() __builtin_amdgcn_s_barrier()

template <int BN> struct BMap;
template <> struct BMap<80> {
  static DI int prow(int k) { return k ^ (((k >> 3) & 1) << 2); }
  static DI int fseg(int k) { return 0; }
};
template <> struct BMap<96> {
  static DI int prow(int k) { return k; }
  static DI int fseg(int k) { return (k >> 3) & 1; }
};
template <> struct BMap<192> {
  static DI int prow(int k) { return k; }
  static DI int fseg(int k) { return ((k >> 1) & 1) | (((k >> 3) & 1) << 1); }
};

template <int BN> DI int boff_lo(int cb, int lane) {
  const int g = lane >> 4, q = (lane >> 2) & 3, p = lane & 3, k = 8 * g + q;
  return BMap<BN>::prow(k) * (2 * BN) + ((cb ^ BMap<BN>::fseg(k)) * 32) + p * 8;
}
template <int BN> DI int boff_hi(int cb, int lane) {
  const int g = lane >> 4, q = (lane >> 2) & 3, p = lane & 3, k = 8 * g + q + 4;
  return BMap<BN>::prow(k) * (2 * BN) + ((cb ^ BMap<BN>::fseg(k)) * 32) + p * 8;
}
DI bf16x8 read_b2(const char LAS* plo, const char LAS* phi) {
  s16x4 lo = tr_read(plo), hi = tr_read(phi);
  return __builtin_shufflevector(lo, hi, 0, 1, 2, 3, 4, 5, 6, 7);
}
DI bf16x8 read_afrag(const char LAS* img, int row, int c) {
  return *(const bf16x8 LAS*)(img + row * 128 + ((c ^ ((row >> 1) & 7)) * 16));
}
DI void st8(void* p, u32x2 v) { *(u32x2*)p = v; }
template <int I, int N, class F> DI void sfor(F&& f) {
  if constexpr (I < N) {
    f(std::integral_constant<int, I>{});
    sfor<I + 1, N>(f);
  }
}
template <int OFF> DI void a_tr(s16x4& d, unsigned addr) {
  asm volatile("ds_read_b64_tr_b16 %0, %1 offset:%2" : "=v"(d) : "v"(addr), "i"(OFF));
}
template <int OFF> DI void a_b128(bf16x8& d, unsigned addr) {
  asm volatile("ds_read_b128 %0, %1 offset:%2" : "=v"(d) : "v"(addr), "i"(OFF));
}
template <int N> DI void lgkm_wait() { asm volatile("s_waitcnt lgkmcnt(%0)" ::"i"(N) : "memory"); }
DI void pin(bf16x8& a) { asm volatile("" : "+v"(a)); }
DI void pin(s16x4& a) { asm volatile("" : "+v"(a)); }
DI bf16x8 cat(const s16x4& lo, const s16x4& hi) { return __builtin_shufflevector(lo, hi, 0, 1, 2, 3, 4, 5, 6, 7); }
DI unsigned ldsaddr(char LAS* p) { return (unsigned)(size_t)p; }
DI bf16x8 ones8() {
  bf16x8 o;
#pragma unroll
  for (int j = 0; j < 8; ++j) o[j] = 0x3F80;
  return o;
}
template <int N> DI void wait_stage(int pend) {
  if (pend >= 3) WAIT_V(3 * N);
  else if (pend == 2) WAIT_V(2 * N);
  else if (pend == 1) WAIT_V(N);
  else WAIT_V(0);
}

template <int BN, int NST, int NIT, bool STATS>
struct SmallGemm {
  static constexpr int CPR = BN / 8;
  static constexpr int BPIECES = 128 * BN * 2 / 1024;
  static constexpr int NI = (16 + BPIECES) / 4;
  static constexpr int STAGE = (16 + BPIECES) * 1024;
  static constexpr int NCB = BN / 16;

  static DI void run(char LAS* lds, const u16* A, int lda, int row0, const u16* B, int ldb, int n0, int nmax,
                     int rot, f32x4 (&acc)[NCB], f32x4& rs, f32x4& sq, f32x4& cs0, f32x4& cs1) {
    const int tid = threadIdx.x, lane = tid & 63;
    const int w = __builtin_amdgcn_readfirstlane(tid >> 6);
    const char* gp[NI];
#pragma unroll
    for (int i = 0; i < NI; ++i) {
      const int pi = i * 4 + w;
      if (i < 4) {
        const int sub = pi >> 3, row = 8 * (pi & 7) + (lane >> 3);
        const int c = (lane & 7) ^ ((row >> 1) & 7);
        gp[i] = (const char*)(A + (size_t)(row0 + row) * lda + sub * 64 + c * 8);
      } else {
        const int ci = (pi - 16) * 64 + lane;
        const int pr = ci / CPR, pc = ci % CPR;
        const int k = BMap<BN>::prow(pr);
        const int c = pc ^ (BMap<BN>::fseg(k) << 1);
        int n = n0 + c * 8;
        n = n > nmax - 8 ? nmax - 8 : n;
        gp[i] = (const char*)(B + (size_t)k * ldb + n);
      }
    }
    const int bstep = 128 * ldb * 2;
    auto issue = [&](int st) {
      char LAS* dst = lds + (st % NST) * STAGE + w * 1024;
#pragma unroll
      for (int i = 0; i < NI; ++i)
        glds16(gp[i] + (size_t)st * (i < 4 ? 256 : bstep), dst + i * 4096);
    };
#pragma unroll
    for (int s = 0; s < NST - 1; ++s) issue(s);
    const int arow = 16 * w + (lane & 15), g = lane >> 4;
    unsigned blo[NCB], bhi[NCB];
#pragma unroll
    for (int p = 0; p < NCB; ++p) {
      int cb = p + rot;
      cb = cb >= NCB ? cb - NCB : cb;
      blo[p] = ldsaddr(lds) + 16384 + boff_lo<BN>(cb, lane);
      bhi[p] = ldsaddr(lds) + 16384 + boff_hi<BN>(cb, lane);
    }
    const unsigned aa0 = ldsaddr(lds) + arow * 128 + ((g ^ ((arow >> 1) & 7)) * 16);
    const unsigned aa1 = ldsaddr(lds) + arow * 128 + (((4 + g) ^ ((arow >> 1) & 7)) * 16);
    const bf16x8 ones = ones8();
    bf16x8 af[2];
    s16x4 bl[2][NCB], bh[2][NCB];
    sfor<0, NIT>([&](auto tc) {
      constexpr int t = decltype(tc)::value;
      constexpr int issued = (t + NST - 1 < NIT) ? (t + NST - 1) : NIT;
      wait_stage<NI>(issued - t - 1);
      BAR();
      if constexpr (t + NST - 1 < NIT) issue(t + NST - 1);
      const unsigned so = (t % NST) * STAGE;
      auto rd = [&](auto kc) {
        constexpr int kk = decltype(kc)::value;
        a_b128<(kk >> 1) * 8192>(af[kk & 1], ((kk & 1) ? aa1 : aa0) + so);
#pragma unroll
        for (int p = 0; p < NCB; ++p) {
          a_tr<kk * 32 * 2 * BN>(bl[kk & 1][p], blo[p] + so);
          a_tr<kk * 32 * 2 * BN>(bh[kk & 1][p], bhi[p] + so);
        }
      };
      rd(std::integral_constant<int, 0>{});
      sfor<0, 4>([&](auto kc) {
        constexpr int kk = decltype(kc)::value;
        if constexpr (kk < 3) {
          rd(std::integral_constant<int, kk + 1>{});
          lgkm_wait<1 + 2 * NCB>();
        } else {
          lgkm_wait<0>();
        }
        pin(af[kk & 1]);
#pragma unroll
        for (int p = 0; p < NCB; ++p) { pin(bl[kk & 1][p]); pin(bh[kk & 1][p]); }
        if (STATS) {
          rs = MFMA16(ones, af[kk & 1], rs);
          sq = MFMA16(af[kk & 1], af[kk & 1], sq);
        }
#pragma unroll
        for (int p = 0; p < NCB; ++p) {
          const bf16x8 bfr = cat(bl[kk & 1][p], bh[kk & 1][p]);
          acc[p] = MFMA16(bfr, af[kk & 1], acc[p]);
          if (STATS && p == 0) cs0 = MFMA16(bfr, ones, cs0);
          if (STATS && p == 4) cs1 = MFMA16(bfr, ones, cs1);
        }
        __builtin_amdgcn_sched_barrier(0);
      });
    });
  }
};

DI void finish_stats(const f32x4& rs, const f32x4& sq, int lane, float& mean, float& rstd) {
  const int m = lane & 15;
  const int r = m & 3;
  float d = r == 0 ? sq[0] : (r == 1 ? sq[1] : (r == 2 ? sq[2] : sq[3]));
  d = __shfl(d, (m >> 2) * 16 + m);
  ln_stats(rs[0], d, mean, rstd);
}

constexpr int K1_LDS = 4 * 40960;
__global__ __launch_bounds__(256, 1) void k1_qkv(const u16* __restrict__ x, const u16* __restrict__ wq,
                                                  const u16* __restrict__ wk, const u16* __restrict__ wv,
                                                  char* __restrict__ ws) {
  extern __shared__ __attribute__((aligned(16))) char smem_[];
  char LAS* lds = (char LAS*)smem_;
  const int tid = threadIdx.x, lane = tid & 63;
  const int w = __builtin_amdgcn_readfirstlane(tid >> 6);
  const int bid = blockIdx.x, xcd = bid & 7, idx = bid >> 3;
  const int rt = (xcd >> 1) * 8 + (idx & 7), ct = (xcd & 1) * 4 + (idx >> 3);
  const int row0 = rt * 64;
  const int g = lane >> 4, m = lane & 15;
  f32x4 z4 = {0.f, 0.f, 0.f, 0.f};
  f32x4 rs = z4, sq = z4;
  float LAS* cs_l = (float LAS*)(lds + 3 * 40960);
  if (ct < 7) {
    using G = SmallGemm<96, 3, 5, true>;
    f32x4 acc[6], cs0 = z4, cs1 = z4;
#pragma unroll
    for (int p = 0; p < 6; ++p) acc[p] = z4;
    G::run(lds, x, D_, row0, wq, D_, ct * 96, D_, w, acc, rs, sq, cs0, cs1);
    float mean, rstd;
    finish_stats(rs, sq, lane, mean, rstd);
    if (m == 0) {
      *(f32x4 LAS*)(cs_l + w * 16 + 4 * g) = cs0;
      if (w < 2) *(f32x4 LAS*)(cs_l + (4 + w) * 16 + 4 * g) = cs1;
    }
    __syncthreads();
    u16* q = (u16*)(ws + WS_Q);
    const float sc = rstd * QSCALE;
#pragma unroll
    for (int p = 0; p < 6; ++p) {
      int cb = p + w;
      cb = cb >= 6 ? cb - 6 : cb;
      const int n = ct * 96 + cb * 16 + 4 * g;
      const f32x4 c = *(const f32x4 LAS*)(cs_l + cb * 16 + 4 * g);
      if (n < D_) {
        u32x2 o;
        o[0] = pk_bf16((acc[p][0] - mean * c[0]) * sc, (acc[p][1] - mean * c[1]) * sc);
        o[1] = pk_bf16((acc[p][2] - mean * c[2]) * sc, (acc[p][3] - mean * c[3]) * sc);
        st8(q + (size_t)(row0 + 16 * w + m) * D_ + n, o);
      }
    }
  } else {
    constexpr int NI = 7, STAGE = 28 * 1024, NST = 4, NIT = 5;
    const char* gp[NI];
#pragma unroll
    for (int i = 0; i < NI; ++i) {
      const int pi = i * 4 + w;
      if (i < 4) {
        const int sub = pi >> 3, row = 8 * (pi & 7) + (lane >> 3);
        const int c = (lane & 7) ^ ((row >> 1) & 7);
        gp[i] = (const char*)(x + (size_t)(row0 + row) * D_ + sub * 64 + c * 8);
      } else {
        int pj = pi - 16;
        pj = pj >= 10 ? pj - 10 : pj;
        const u16* src = pj < 5 ? wk : wv;
        gp[i] = (const char*)src + (pj < 5 ? pj : pj - 5) * 1024 + lane * 16;
      }
    }
    auto issue = [&](int st) {
      char LAS* dst = lds + (st % NST) * STAGE + w * 1024;
#pragma unroll
      for (int i = 0; i < NI; ++i) glds16(gp[i] + (size_t)st * (i < 4 ? 256 : 5120), dst + i * 4096);
    };
#pragma unroll
    for (int s = 0; s < NST - 1; ++s) issue(s);
    const int arow = 16 * w + m;
    const int q4 = (lane >> 2) & 3, p4 = lane & 3;
    const int bo = (8 * g + q4) * 40 + p4 * 8;
    f32x4 ak0 = z4, ak1 = z4, av0 = z4, av1 = z4, ck0 = z4, ck1 = z4, cv0 = z4, cv1 = z4;
    const bf16x8 ones = ones8();
    const unsigned aa0 = ldsaddr(lds) + arow * 128 + ((g ^ ((arow >> 1) & 7)) * 16);
    const unsigned aa1 = ldsaddr(lds) + arow * 128 + (((4 + g) ^ ((arow >> 1) & 7)) * 16);
    const unsigned bb = ldsaddr(lds) + 16384 + bo;
    sfor<0, NIT>([&](auto tc) {
      constexpr int t = decltype(tc)::value;
      constexpr int issued = (t + NST - 1 < NIT) ? (t + NST - 1) : NIT;
      wait_stage<NI>(issued - t - 1);
      BAR();
      if constexpr (t + NST - 1 < NIT) issue(t + NST - 1);
      const unsigned so = (t % NST) * STAGE;
      sfor<0, 4>([&](auto kc) {
        constexpr int kk = decltype(kc)::value;
        bf16x8 af;
        s16x4 r[8];
        a_b128<(kk >> 1) * 8192>(af, ((kk & 1) ? aa1 : aa0) + so);
        constexpr int o = kk * 32 * 40;
        a_tr<o>(r[0], bb + so); a_tr<o + 160>(r[1], bb + so); a_tr<o + 32>(r[2], bb + so); a_tr<o + 192>(r[3], bb + so);
        a_tr<5120 + o>(r[4], bb + so); a_tr<5120 + o + 160>(r[5], bb + so);
        a_tr<5120 + o + 32>(r[6], bb + so); a_tr<5120 + o + 192>(r[7], bb + so);
        lgkm_wait<0>();
        pin(af);
#pragma unroll
        for (int j = 0; j < 8; ++j) pin(r[j]);
        const bf16x8 k0 = cat(r[0], r[1]), k1 = cat(r[2], r[3]), v0 = cat(r[4], r[5]), v1 = cat(r[6], r[7]);
        rs = MFMA16(ones, af, rs);
        sq = MFMA16(af, af, sq);
        ak0 = MFMA16(k0, af, ak0);
        ak1 = MFMA16(k1, af, ak1);
        ck0 = MFMA16(k0, ones, ck0);
        ck1 = MFMA16(k1, ones, ck1);
        av0 = MFMA16(af, v0, av0);
        av1 = MFMA16(af, v1, av1);
        cv0 = MFMA16(ones, v0, cv0);
        cv1 = MFMA16(ones, v1, cv1);
      });
    });
    float mean, rstd;
    finish_stats(rs, sq, lane, mean, rstd);
    u16* kf = (u16*)(ws + WS_KF);
    u16* vf = (u16*)(ws + WS_VF);
    const int key = row0 + 16 * w + m;
    const int blk = key >> 5, kk5 = key & 31;
    const int r = (kk5 & 19) | ((kk5 & 4) << 1) | ((kk5 & 8) >> 1);
    float kv[4], n2;
    u32x2 o;
#pragma unroll
    for (int j = 0; j < 4; ++j) kv[j] = (ak0[j] - mean * ck0[j]) * rstd;
    o[0] = pk_bf16(kv[0], kv[1]);
    o[1] = pk_bf16(kv[2], kv[3]);
    n2 = bf_lo(o[0]) * bf_lo(o[0]) + bf_hi(o[0]) * bf_hi(o[0]) + bf_lo(o[1]) * bf_lo(o[1]) + bf_hi(o[1]) * bf_hi(o[1]);
    *(u32x2*)(kf + (size_t)blk * 1024 + ((g >> 1) * 32 + r) * 8 + (g & 1) * 4) = o;
    {
      u32x2 o1;
#pragma unroll
      for (int j = 0; j < 4; ++j) kv[j] = (ak1[j] - mean * ck1[j]) * rstd;
      o1[0] = pk_bf16(kv[0], kv[1]);
      o1[1] = pk_bf16(kv[2], kv[3]);
      if (g == 0)
        n2 += bf_lo(o1[0]) * bf_lo(o1[0]) + bf_hi(o1[0]) * bf_hi(o1[0]) + bf_lo(o1[1]) * bf_lo(o1[1]) +
              bf_hi(o1[1]) * bf_hi(o1[1]);
      if (g == 1) { o1[0] = 0x3F803F80u; o1[1] = 0x00003F80u; }
      if (g >= 2) { o1[0] = 0u; o1[1] = 0u; }
      *(u32x2*)(kf + (size_t)blk * 1024 + 512 + ((g >> 1) * 32 + r) * 8 + (g & 1) * 4) = o1;
    }
    n2 += __shfl_xor(n2, 16);
    n2 += __shfl_xor(n2, 32);
#pragma unroll
    for (int s = 1; s < 16; s <<= 1) n2 = fmaxf(n2, __shfl_xor(n2, s));
    float LAS* red = (float LAS*)(lds + 4 * 28 * 1024);
    if (lane == 0) red[w] = n2;
    {
      float mj[4], rj[4];
#pragma unroll
      for (int j = 0; j < 4; ++j) {
        mj[j] = __shfl(mean, 4 * g + j);
        rj[j] = __shfl(rstd, 4 * g + j);
      }
      const int key0 = row0 + 16 * w + 4 * g;
      const int vb = key0 >> 5, k5 = key0 & 31;
      const int s = k5 >> 4, h = (k5 >> 3) & 1, j0 = k5 & 7;
      u16* base = vf + (size_t)vb * 1024 + s * 512 + h * 256 + j0;
      u32x2 ov;
      ov[0] = pk_bf16((av0[0] - mj[0] * cv0[0]) * rj[0], (av0[1] - mj[1] * cv0[1]) * rj[1]);
      ov[1] = pk_bf16((av0[2] - mj[2] * cv0[2]) * rj[2], (av0[3] - mj[3] * cv0[3]) * rj[3]);
      *(u32x2*)(base + m * 8) = ov;
      ov[0] = pk_bf16((av1[0] - mj[0] * cv1[0]) * rj[0], (av1[1] - mj[1] * cv1[1]) * rj[1]);
      ov[1] = pk_bf16((av1[2] - mj[2] * cv1[2]) * rj[2], (av1[3] - mj[3] * cv1[3]) * rj[3]);
      if (m == 4) { ov[0] = 0x3F803F80u; ov[1] = 0x3F803F80u; }
      if (m > 4) { ov[0] = 0u; ov[1] = 0u; }
      *(u32x2*)(base + (16 + m) * 8) = ov;
    }
    __syncthreads();
    if (tid == 0) ((float*)(ws + WS_KMX))[rt] = fmaxf(fmaxf(red[0], red[1]), fmaxf(red[2], red[3]));
  }
}

#define K2_NW 8
constexpr int K2_KS = K2_NW / 2, K2_NB = 64 / K2_KS, K2_T = K2_NW * 64;
constexpr int K2_LDS = 4 * 256 * 96 + 4096;
DI void split3(float M, unsigned& s01, unsigned& s2) {
  const float n = -M;
  const unsigned hi = __float_as_uint(n) & 0xffff0000u;
  const float r1 = n - __uint_as_float(hi);
  const unsigned mid = __float_as_uint(r1) & 0xffff0000u;
  const float r2 = r1 - __uint_as_float(mid);
  const unsigned lo = __float_as_uint(r2) & 0xffff0000u;
  s01 = (hi >> 16) | mid;
  s2 = lo >> 16;
}
__global__ __launch_bounds__(K2_T, 1) void k2_attn(const char* __restrict__ wsr, char* __restrict__ ws,
                                                   const char* __restrict__ wo, const char* __restrict__ wg,
                                                   const char* __restrict__ wu, const char* __restrict__ wd) {
  extern __shared__ __attribute__((aligned(16))) char smem_[];
  char LAS* lds = (char LAS*)smem_;
  const int tid = threadIdx.x, lane = tid & 63;
  const int w = __builtin_amdgcn_readfirstlane(tid >> 6);
  const int ch = w & 1, ks = w >> 1;
  const int r = lane & 31, h = lane >> 5;
  const int qrow0 = blockIdx.x * 8;
  const u16* q = (const u16*)(wsr + WS_Q);
  const bf16x8* kf = (const bf16x8*)(wsr + WS_KF) + (size_t)ks * K2_NB * 128 + lane;
  const bf16x8* vf = (const bf16x8*)(wsr + WS_VF) + (size_t)ks * K2_NB * 128 + lane;
  bf16x8 kA0, kA1, vA0, vA1, kB0, kB1, vB0, vB1;
  auto ld2 = [&](bf16x8& a0, bf16x8& a1, const bf16x8* p) {
    asm volatile("global_load_dwordx4 %0, %1, off" : "=v"(a0) : "v"(p));
    asm volatile("global_load_dwordx4 %0, %1, off offset:1024" : "=v"(a1) : "v"(p));
  };
  ld2(kA0, kA1, kf);
  ld2(vA0, vA1, vf);
  ld2(kB0, kB1, kf + 128);
  bf16x8 q0[4], q1[4];
  float qn[4];
#pragma unroll
  for (int gq = 0; gq < 4; ++gq) {
    const u16* src = q + (size_t)(qrow0 + ch * 4 + gq) * D_ + r * 20;
    const u32x2 a = *(const u32x2*)(src + 8 * h), b = *(const u32x2*)(src + 8 * h + 4);
    u32x2 c = *(const u32x2*)(src + 16);
    if (h) { c[0] = 0u; c[1] = 0u; }
    u32x4 f0 = {a[0], a[1], b[0], b[1]};
    u32x4 f1 = {c[0], c[1], 0u, 0u};
    float n2 = 0.f;
#pragma unroll
    for (int e = 0; e < 4; ++e) n2 += bf_lo(f0[e]) * bf_lo(f0[e]) + bf_hi(f0[e]) * bf_hi(f0[e]);
#pragma unroll
    for (int e = 0; e < 2; ++e) n2 += bf_lo(f1[e]) * bf_lo(f1[e]) + bf_hi(f1[e]) * bf_hi(f1[e]);
    n2 += __shfl_xor(n2, 32);
    qn[gq] = n2;
    q0[gq] = __builtin_bit_cast(bf16x8, f0);
    q1[gq] = __builtin_bit_cast(bf16x8, f1);
  }
  float kmx = ((const float*)(wsr + WS_KMX))[r];
#pragma unroll
  for (int s = 1; s < 32; s <<= 1) kmx = fmaxf(kmx, __shfl_xor(kmx, s));
  float M[4];
  bool unsafe = false;
#pragma unroll
  for (int gq = 0; gq < 4; ++gq) {
    M[gq] = sqrtf(qn[gq] * kmx) * 1.001f + 1e-3f;
    unsafe |= !(M[gq] <= FAST_MAX);
  }
  f32x16 z16;
#pragma unroll
  for (int i = 0; i < 16; ++i) z16[i] = 0.f;
  if (__any(unsafe)) {
    const bf16x8* kall = (const bf16x8*)(wsr + WS_KF) + lane;
    float mx[4] = {-INFINITY, -INFINITY, -INFINITY, -INFINITY};
    for (int b = 0; b < 64; ++b) {
      const bf16x8 k0 = kall[b * 128], k1 = kall[b * 128 + 64];
#pragma unroll
      for (int gq = 0; gq < 4; ++gq) {
        f32x16 s = MFMA32(k0, q0[gq], z16);
        s = MFMA32(k1, q1[gq], s);
#pragma unroll
        for (int i = 0; i < 16; ++i) mx[gq] = fmaxf(mx[gq], s[i]);
      }
    }
#pragma unroll
    for (int gq = 0; gq < 4; ++gq) M[gq] = fmaxf(mx[gq], __shfl_xor(mx[gq], 32));
  }
#pragma unroll
  for (int gq = 0; gq < 4; ++gq) {
    unsigned s01, s2;
    split3(M[gq], s01, s2);
    u32x4 f1 = __builtin_bit_cast(u32x4, q1[gq]);
    if (h == 0) { f1[2] = s01; f1[3] = s2; }
    q1[gq] = __builtin_bit_cast(bf16x8, f1);
  }
  f32x16 o[4];
#pragma unroll
  for (int gq = 0; gq < 4; ++gq) o[gq] = z16;
  {
    f32x16 sA, sB;
    u32x4 p0, p1 = {0u, 0u, 0u, 0u};
    float eA[4] = {0.f, 0.f, 0.f, 0.f}, eB[4];
#define SB() __builtin_amdgcn_sched_barrier(0)
#define VW2(n, a, b) asm volatile("s_waitcnt vmcnt(" #n ")" : "+v"(a), "+v"(b))
#define EX4(e, sc, i)                                                                    \
    e[0] = __builtin_amdgcn_exp2f(sc[i]); e[1] = __builtin_amdgcn_exp2f(sc[i + 1]);       \
    e[2] = __builtin_amdgcn_exp2f(sc[i + 2]); e[3] = __builtin_amdgcn_exp2f(sc[i + 3]);
#define CV2(dst, j, e) dst[j] = pk_bf16(e[0], e[1]); dst[j + 1] = pk_bf16(e[2], e[3]);
#define SLOT(sc, sn, nk0, nk1, gn, gp, pvv1, gq, cv0, PRE1, POST2, POST3, PRE4) \
    PRE1                                                                        \
    sn = MFMA32(nk0, q0[gn], z16); SB();                                        \
    CV2(p1, 2, eA) SB(); EX4(eB, sc, 0) SB();                                   \
    o[gp] = MFMA32(pvv1, __builtin_bit_cast(bf16x8, p1), o[gp]); SB();          \
    POST2                                                                       \
    CV2(p0, 0, eB) SB(); EX4(eA, sc, 4) SB();                                   \
    sn = MFMA32(nk1, q1[gn], sn); SB();                                         \
    POST3                                                                       \
    CV2(p0, 2, eA) SB(); EX4(eB, sc, 8) SB();                                   \
    PRE4                                                                        \
    o[gq] = MFMA32(cv0, __builtin_bit_cast(bf16x8, p0), o[gq]); SB();           \
    CV2(p1, 0, eB) SB(); EX4(eA, sc, 12) SB();
#define BLOCK(ck0, ck1, cv0, cv1, nk0, nk1, nv0, nv1, b1, b2)                                             \
    SLOT(sA, sB, ck0, ck1, 1, 3, nv1, 0, cv0, , ld2(nv0, nv1, vf + (b1) * 128);, , VW2(4, cv0, cv1);)     \
    SLOT(sB, sA, ck0, ck1, 2, 0, cv1, 1, cv0, , , , )                                                     \
    SLOT(sA, sB, ck0, ck1, 3, 1, cv1, 2, cv0, , , ld2(ck0, ck1, kf + (b2) * 128);, )                      \
    SLOT(sB, sA, nk0, nk1, 0, 2, cv1, 3, cv0, VW2(4, nk0, nk1);, , , )
#pragma unroll
    for (int j = 0; j < 8; ++j) vB1[j] = 0;
    asm volatile("s_waitcnt vmcnt(0)" : "+v"(kA0), "+v"(kA1), "+v"(vA0), "+v"(vA1), "+v"(kB0), "+v"(kB1));
    sA = MFMA32(kA0, q0[0], z16);
    sA = MFMA32(kA1, q1[0], sA);
    for (int b = 0; b < K2_NB; b += 2) {
      BLOCK(kA0, kA1, vA0, vA1, kB0, kB1, vB0, vB1, b + 1, (b + 2 < K2_NB ? b + 2 : K2_NB - 1))
      BLOCK(kB0, kB1, vB0, vB1, kA0, kA1, vA0, vA1, (b + 2 < K2_NB ? b + 2 : K2_NB - 1), (b + 3 < K2_NB ? b + 3 : K2_NB - 1))
    }
    CV2(p1, 2, eA)
    o[3] = MFMA32(vB1, __builtin_bit_cast(bf16x8, p1), o[3]);
    asm volatile("s_waitcnt vmcnt(0)" ::: "memory");
  }
  if (w < 4) {
    constexpr int NSLOT = 1024, NPC = 3 * 1920 + 800, NIT = (NPC + NSLOT - 1) / NSLOT;
    const int slot = blockIdx.x * 4 + w;
    unsigned acc_ = 0;
#pragma unroll 8
    for (int i = 0; i < NIT; ++i) {
      int pi = slot + i * NSLOT;
      pi = pi < NPC ? pi : slot;
      const char* base = pi < 1920 ? wd : (pi < 3840 ? wg : (pi < 5760 ? wu : wo));
      const int off = pi < 1920 ? pi : (pi < 3840 ? pi - 1920 : (pi < 5760 ? pi - 3840 : pi - 5760));
      const u32x4 v = *(const u32x4*)(base + (size_t)off * 1024 + lane * 16);
      acc_ ^= v[0];
    }
    asm volatile("" ::"v"(acc_));
  }
  float LAS* red = (float LAS*)lds;
#pragma unroll
  for (int gq = 0; gq < 4; ++gq) {
    float LAS* dst = red + ((size_t)ks * 256 + ch * 128 + gq * 32 + r) * 24 + 4 * h;
#pragma unroll
    for (int t = 0; t < 3; ++t) {
      f32x4 v = {o[gq][4 * t], o[gq][4 * t + 1], o[gq][4 * t + 2], o[gq][4 * t + 3]};
      *(f32x4 LAS*)(dst + 8 * t) = v;
    }
  }
  __syncthreads();
  u16* og = (u16*)(ws + WS_O) + (size_t)qrow0 * D_;
  for (int it = tid; it < 1280; it += K2_T) {
    const int c = it / 5, qd = it % 5;
    f32x4 a = {0.f, 0.f, 0.f, 0.f};
    float l = 0.f;
#pragma unroll
    for (int s = 0; s < K2_KS; ++s) {
      const float LAS* src = red + ((size_t)s * 256 + c) * 24;
      a += *(const f32x4 LAS*)(src + 4 * qd);
      l += src[20];
    }
    const float inv = 1.0f / l;
    u32x2 ov;
    ov[0] = pk_bf16(a[0] * inv, a[1] * inv);
    ov[1] = pk_bf16(a[2] * inv, a[3] * inv);
    st8(og + (size_t)it * 4, ov);
  }
}

template <int KDIM, bool STATS_OUT>
__global__ __launch_bounds__(256, 1) void k3_proj(const u16* __restrict__ A, const u16* __restrict__ W,
                                                   const u16* __restrict__ res, u16* __restrict__ out,
                                                   float* __restrict__ stats) {
  extern __shared__ __attribute__((aligned(16))) char smem_[];
  char LAS* lds = (char LAS*)smem_;
  const int tid = threadIdx.x, lane = tid & 63;
  const int w = __builtin_amdgcn_readfirstlane(tid >> 6);
  const int bid = blockIdx.x, xcd = bid & 7, idx = bid >> 3;
  const int rt = (xcd >> 1) * 8 + (idx & 7), ct = (xcd & 1) * 4 + (idx >> 3);
  const int g = lane >> 4, m = lane & 15;
  const int row = rt * 64 + 16 * w + m;
  u32x2 rv[5];
#pragma unroll
  for (int p = 0; p < 5; ++p) rv[p] = *(const u32x2*)(res + (size_t)row * D_ + ct * 80 + p * 16 + 4 * g);
  using G = SmallGemm<80, 4, KDIM / 128, false>;
  f32x4 z4 = {0.f, 0.f, 0.f, 0.f};
  f32x4 acc[5], d0 = z4, d1 = z4, d2 = z4, d3 = z4;
#pragma unroll
  for (int p = 0; p < 5; ++p) acc[p] = z4;
  G::run(lds, A, KDIM, rt * 64, W, D_, ct * 80, D_, 0, acc, d0, d1, d2, d3);
  float s1 = 0.f, s2 = 0.f;
#pragma unroll
  for (int p = 0; p < 5; ++p) {
    u32x2 o;
    o[0] = pk_bf16(bf_lo(rv[p][0]) + acc[p][0], bf_hi(rv[p][0]) + acc[p][1]);
    o[1] = pk_bf16(bf_lo(rv[p][1]) + acc[p][2], bf_hi(rv[p][1]) + acc[p][3]);
    if (STATS_OUT) st8(out + (size_t)row * D_ + ct * 80 + p * 16 + 4 * g, o);
    else *(u32x2*)(out + (size_t)row * D_ + ct * 80 + p * 16 + 4 * g) = o;
    if (STATS_OUT) {
      const float a = bf_lo(o[0]), b = bf_hi(o[0]), c = bf_lo(o[1]), d = bf_hi(o[1]);
      s1 += (a + b) + (c + d);
      s2 += (a * a + b * b) + (c * c + d * d);
    }
  }
  if (STATS_OUT) {
    s1 += __shfl_xor(s1, 16); s1 += __shfl_xor(s1, 32);
    s2 += __shfl_xor(s2, 16); s2 += __shfl_xor(s2, 32);
    if (g == 0) { f32x2 v = {s1, s2}; *(f32x2*)(stats + ((size_t)row * 8 + ct) * 2) = v; }
  }
}

constexpr int K4_STAGE = 40960, K4_LDS = 4 * K4_STAGE;
DI float gelu_tanh(float x) {
  constexpr float C0 = -2.0f * 0.7978845608028654f * 1.4426950408889634f, C1 = C0 * 0.044715f;
  const float e = __builtin_amdgcn_exp2f(x * __builtin_fmaf(x * x, C1, C0));
  return x * __builtin_amdgcn_rcpf(1.0f + e);
}
__global__ __launch_bounds__(512, 2) void k4_ffn1(const u16* __restrict__ hh, const u16* __restrict__ wg,
                                                   const u16* __restrict__ wu, const float* __restrict__ stats,
                                                   u16* __restrict__ gout) {
  extern __shared__ __attribute__((aligned(16))) char smem_[];
  char LAS* lds = (char LAS*)smem_;
  const int tid = threadIdx.x, lane = tid & 63;
  const int w = __builtin_amdgcn_readfirstlane(tid >> 6);
  const int bid = blockIdx.x, xcd = bid & 7, idx = bid >> 3;
  const int rt = (xcd >> 2) * 8 + (idx & 7), ct = (xcd & 3) * 4 + (idx >> 3);
  const int row0 = rt * 128, f0 = ct * 96;
  const int wr = w >> 1, wc = w & 1;
  const int g = lane >> 4, m = lane & 15;
  constexpr int NI = 5, NST = 4, NIT = 10;
  f32x4 st[2][4];
#pragma unroll
  for (int rb = 0; rb < 2; ++rb)
#pragma unroll
    for (int j = 0; j < 4; ++j)
      st[rb][j] = *(const f32x4*)(stats + ((size_t)(row0 + 32 * wr + 16 * rb + m) * 8 + 2 * j) * 2);
  const int rot = wr >= 3 ? 0 : wr;
  const char* gp[NI];
#pragma unroll
  for (int i = 0; i < NI; ++i) {
    const int pi = i * 8 + w;
    if (i < 2) {
      const int row = 8 * pi + (lane >> 3);
      const int c = (lane & 7) ^ ((row >> 1) & 7);
      gp[i] = (const char*)(hh + (size_t)(row0 + row) * D_ + c * 8);
    } else {
      const int ci = (pi - 16) * 64 + lane;
      const int pr = ci / 24, pc = ci % 24;
      const int c = pc ^ (BMap<192>::fseg(pr) << 1);
      const u16* src = c < 12 ? wg + f0 + c * 8 : wu + f0 + (c - 12) * 8;
      gp[i] = (const char*)(src + (size_t)pr * F_);
    }
  }
  auto issue = [&](int st_) {
    char LAS* dst = lds + (st_ % NST) * K4_STAGE + w * 1024;
#pragma unroll
    for (int i = 0; i < NI; ++i) glds16(gp[i] + (size_t)st_ * (i < 2 ? 128 : 64 * F_ * 2), dst + i * 8192);
  };
#pragma unroll
  for (int s = 0; s < NST - 1; ++s) issue(s);
  unsigned blo[6], bhi[6];
#pragma unroll
  for (int p = 0; p < 6; ++p) {
    int c3 = (p % 3) + rot;
    c3 = c3 >= 3 ? c3 - 3 : c3;
    const int cb = (p < 3 ? 0 : 6) + 3 * wc + c3;
    blo[p] = ldsaddr(lds) + 16384 + boff_lo<192>(cb, lane);
    bhi[p] = ldsaddr(lds) + 16384 + boff_hi<192>(cb, lane);
  }
  const int ar = 32 * wr + m;
  const unsigned aa0 = ldsaddr(lds) + ar * 128 + ((g ^ ((ar >> 1) & 7)) * 16);
  const unsigned aa1 = ldsaddr(lds) + ar * 128 + (((4 + g) ^ ((ar >> 1) & 7)) * 16);
  f32x4 acc[2][6];
#pragma unroll
  for (int rb = 0; rb < 2; ++rb)
#pragma unroll
    for (int p = 0; p < 6; ++p) acc[rb][p] = f32x4{0.f, 0.f, 0.f, 0.f};
  bf16x8 a0[2], a1[2];
  s16x4 bl[2][6], bh[2][6];
  f32x4 cg = {0.f, 0.f, 0.f, 0.f}, cu = {0.f, 0.f, 0.f, 0.f};
  const bf16x8 ones = ones8();
  sfor<0, NIT>([&](auto tc) {
    constexpr int t = decltype(tc)::value;
    constexpr int issued = (t + NST - 1 < NIT) ? (t + NST - 1) : NIT;
    wait_stage<NI>(issued - t - 1);
    BAR();
    if constexpr (t + NST - 1 < NIT) issue(t + NST - 1);
    const unsigned so = (t % NST) * K4_STAGE;
    auto rd = [&](auto kc) {
      constexpr int kk = decltype(kc)::value;
      a_b128<0>(a0[kk], (kk ? aa1 : aa0) + so);
      a_b128<2048>(a1[kk], (kk ? aa1 : aa0) + so);
#pragma unroll
      for (int p = 0; p < 6; ++p) {
        a_tr<kk * 32 * 384>(bl[kk][p], blo[p] + so);
        a_tr<kk * 32 * 384>(bh[kk][p], bhi[p] + so);
      }
    };
    rd(std::integral_constant<int, 0>{});
    rd(std::integral_constant<int, 1>{});
    sfor<0, 2>([&](auto kc) {
      constexpr int kk = decltype(kc)::value;
      if constexpr (kk == 0) lgkm_wait<14>(); else lgkm_wait<0>();
      pin(a0[kk]); pin(a1[kk]);
#pragma unroll
      for (int p = 0; p < 6; ++p) { pin(bl[kk][p]); pin(bh[kk][p]); }
#pragma unroll
      for (int p = 0; p < 6; ++p) {
        const bf16x8 bfr = cat(bl[kk][p], bh[kk][p]);
        acc[0][p] = MFMA16(bfr, a0[kk], acc[0][p]);
        acc[1][p] = MFMA16(bfr, a1[kk], acc[1][p]);
        if (p == 0) cg = MFMA16(bfr, ones, cg);
        if (p == 3) cu = MFMA16(bfr, ones, cu);
      }
      __builtin_amdgcn_sched_barrier(0);
    });
  });
  float LAS* csl = (float LAS*)(lds + 2 * K4_STAGE);
  if (m == 0 && wr < 3) {
    *(f32x4 LAS*)(csl + wc * 96 + rot * 16 + 4 * g) = cg;
    *(f32x4 LAS*)(csl + wc * 96 + 48 + rot * 16 + 4 * g) = cu;
  }
  __syncthreads();
  f32x4 csg[3], csu[3];
#pragma unroll
  for (int p = 0; p < 3; ++p) {
    int c3 = p + rot;
    c3 = c3 >= 3 ? c3 - 3 : c3;
    csg[p] = *(const f32x4 LAS*)(csl + wc * 96 + c3 * 16 + 4 * g);
    csu[p] = *(const f32x4 LAS*)(csl + wc * 96 + 48 + c3 * 16 + 4 * g);
  }
#pragma unroll
  for (int rb = 0; rb < 2; ++rb) {
    const float s1 = (st[rb][0][0] + st[rb][0][2]) + (st[rb][1][0] + st[rb][1][2]) + (st[rb][2][0] + st[rb][2][2]) + (st[rb][3][0] + st[rb][3][2]);
    const float s2 = (st[rb][0][1] + st[rb][0][3]) + (st[rb][1][1] + st[rb][1][3]) + (st[rb][2][1] + st[rb][2][3]) + (st[rb][3][1] + st[rb][3][3]);
    float mean, rstd;
    ln_stats(s1, s2, mean, rstd);
    u16* dst = gout + (size_t)(row0 + 32 * wr + 16 * rb + m) * F_ + f0 + 48 * wc + 4 * g;
    const f32x2 rs2 = {rstd, rstd}, nm2 = {-mean, -mean};
#pragma unroll
    for (int p = 0; p < 3; ++p) {
      u32x2 o;
#pragma unroll
      for (int hf = 0; hf < 2; ++hf) {
        const f32x2 ag = {acc[rb][p][2 * hf], acc[rb][p][2 * hf + 1]}, au = {acc[rb][p + 3][2 * hf], acc[rb][p + 3][2 * hf + 1]};
        const f32x2 c2g = {csg[p][2 * hf], csg[p][2 * hf + 1]}, c2u = {csu[p][2 * hf], csu[p][2 * hf + 1]};
        const f32x2 ga = __builtin_elementwise_fma(nm2, c2g, ag) * rs2;
        const f32x2 up = __builtin_elementwise_fma(nm2, c2u, au) * rs2;
        constexpr float C0 = -2.0f * 0.7978845608028654f * 1.4426950408889634f, C1 = C0 * 0.044715f;
        const f32x2 k0 = {C0, C0}, k1 = {C1, C1};
        const f32x2 arg = ga * __builtin_elementwise_fma(ga * ga, k1, k0);
        f32x2 den = {__builtin_amdgcn_exp2f(arg[0]), __builtin_amdgcn_exp2f(arg[1])};
        den += f32x2{1.0f, 1.0f};
        const f32x2 sg = {__builtin_amdgcn_rcpf(den[0]), __builtin_amdgcn_rcpf(den[1])};
        const f32x2 r = (ga * sg) * up;
        o[hf] = pk_bf16(r[0], r[1]);
      }
      int c3 = p + rot;
      c3 = c3 >= 3 ? c3 - 3 : c3;
      st8(dst + 16 * c3, o);
    }
  }
}

extern "C" void kernel_launch(void* const* d_in, const int* in_sizes, int n_in, void* d_out, int out_size,
                              void* d_ws, size_t ws_size, hipStream_t stream) {
  const u16* x = (const u16*)d_in[0];
  const u16* wq = (const u16*)d_in[1];
  const u16* wk = (const u16*)d_in[2];
  const u16* wv = (const u16*)d_in[3];
  const u16* wo = (const u16*)d_in[4];
  const u16* wg = (const u16*)d_in[6];
  const u16* wu = (const u16*)d_in[7];
  const u16* wd = (const u16*)d_in[8];
  char* ws = (char*)d_ws;
  constexpr int K3_LDS = 4 * 36864;
  hipFuncSetAttribute((const void*)k1_qkv, hipFuncAttributeMaxDynamicSharedMemorySize, K1_LDS);
  hipFuncSetAttribute((const void*)k2_attn, hipFuncAttributeMaxDynamicSharedMemorySize, K2_LDS);
  hipFuncSetAttribute((const void*)k3_proj<640, true>, hipFuncAttributeMaxDynamicSharedMemorySize, K3_LDS);
  hipFuncSetAttribute((const void*)k3_proj<1536, false>, hipFuncAttributeMaxDynamicSharedMemorySize, K3_LDS);
  hipFuncSetAttribute((const void*)k4_ffn1, hipFuncAttributeMaxDynamicSharedMemorySize, K4_LDS);
  k1_qkv<<<256, 256, K1_LDS, stream>>>(x, wq, wk, wv, ws);
  k2_attn<<<256, K2_T, K2_LDS, stream>>>(ws, ws, (const char*)wo, (const char*)wg, (const char*)wu, (const char*)wd);
  k3_proj<640, true><<<256, 256, K3_LDS, stream>>>((const u16*)(ws + WS_O), wo, x, (u16*)(ws + WS_H),
                                                     (float*)(ws + WS_ST));
  k4_ffn1<<<256, 512, K4_LDS, stream>>>((const u16*)(ws + WS_H), wg, wu, (const float*)(ws + WS_ST),
                                         (u16*)(ws + WS_G));
  k3_proj<1536, false><<<256, 256, K3_LDS, stream>>>((const u16*)(ws + WS_G), wd, (const u16*)(ws + WS_H),
                                                      (u16*)d_out, nullptr);
}
```
